```python
import torch

def _softmax(x, axis=-1):
    return torch.softmax(x, dim=axis)
def _gelu(x):
    return torch.nn.functional.gelu(x, approximate="tanh")
def _silu(x):
    return torch.nn.functional.silu(x)
def _sigmoid(x):
    return torch.sigmoid(x)
def _layernorm(x, axis=-1, eps=1e-5):
    m = x.mean(dim=axis, keepdim=True)
    v = x.var(dim=axis, keepdim=True, unbiased=False)
    return (x - m) / torch.sqrt(v + eps)
def _scatter_add(tgt, idx, src, axis=0):
    return tgt.clone().index_add_(axis, idx, src)
def _segment_sum(src, seg, n):
    out = torch.zeros((n,) + src.shape[1:], dtype=src.dtype)
    return out.index_add_(0, seg, src)
def _pad(x, pw):
    flat = tuple(v for pair in reversed(pw) for v in pair)
    return torch.nn.functional.pad(x, flat)

kernel_name = 'concat_heads__gqa_block_cached__gqa_block_cached__decode'

def setup_inputs():

    _w = (lambda a: (lambda t: t.to(torch.bfloat16) if t.is_floating_point() else t)(torch.as_tensor(a)))
    def _A_setup():
        B, Sq, D = 2, 1, 1024
        Hq, Hkv, Sk, Dh = 32, 4, 32768, 128
        assert Hq % Hkv == 0, (Hq, Hkv)
        return {
            "x": _w((lambda *s: torch.randn(*s, dtype=torch.bfloat16) * 0.8)(B, Sq, D)),
            "k_cache": _w((lambda *s: torch.randn(*s, dtype=torch.bfloat16) * 0.8)(B, Hkv, Sk, Dh)),
            "v_cache": _w((lambda *s: torch.randn(*s, dtype=torch.bfloat16) * 0.05)(B, Hkv, Sk, Dh)),
            "wq": _w((lambda *s: torch.randn(*s, dtype=torch.bfloat16) * 0.8)(D, Hq * Dh) * (D ** -0.5)),
            "wo": _w((lambda *s: torch.randn(*s, dtype=torch.bfloat16) * 0.05)(Hq * Dh, D) * ((Hq * Dh) ** -0.5)),
        }
    _A_in = _A_setup()

    def _B_setup():
        B, Sq, D = 2, 1, 1024
        Hq, Hkv, Sk, Dh = 32, 4, 32768, 128
        assert Hq % Hkv == 0, (Hq, Hkv)
        return {
            "x": _w((lambda *s: torch.randn(*s, dtype=torch.bfloat16) * 0.8)(B, Sq, D)),
            "k_cache": _w((lambda *s: torch.randn(*s, dtype=torch.bfloat16) * 0.8)(B, Hkv, Sk, Dh)),
            "v_cache": _w((lambda *s: torch.randn(*s, dtype=torch.bfloat16) * 0.05)(B, Hkv, Sk, Dh)),
            "wq": _w((lambda *s: torch.randn(*s, dtype=torch.bfloat16) * 0.8)(D, Hq * Dh) * (D ** -0.5)),
            "wo": _w((lambda *s: torch.randn(*s, dtype=torch.bfloat16) * 0.05)(Hq * Dh, D) * ((Hq * Dh) ** -0.5)),
        }
    _B_in = _B_setup()

    return {
        "A_x": _A_in["x"],
        "A_k_cache": _A_in["k_cache"],
        "A_v_cache": _A_in["v_cache"],
        "A_wq": _A_in["wq"],
        "A_wo": _A_in["wo"],
        "B_x": _B_in["x"],
        "B_k_cache": _B_in["k_cache"],
        "B_v_cache": _B_in["v_cache"],
        "B_wq": _B_in["wq"],
        "B_wo": _B_in["wo"],
    }

def reference(A_x, A_k_cache, A_v_cache, A_wq, A_wo, B_x, B_k_cache, B_v_cache, B_wq, B_wo):
    def _A_ref(x, k_cache, v_cache, wq, wo):
        B, Sq, D = x.shape
        Hq, Dh = 32, 128
        Hkv = k_cache.shape[1]
        rep = Hq // Hkv
        q = (lambda x, *d: x.permute(*d))((x @ wq).reshape(B, Sq, Hq, Dh), 0, 2, 1, 3)
        k = (lambda x, n, axis: torch.repeat_interleave(x, n, dim=axis))(k_cache, rep, axis=1)
        v = (lambda x, n, axis: torch.repeat_interleave(x, n, dim=axis))(v_cache, rep, axis=1)
        scores = torch.einsum("bhqd,bhkd->bhqk", q, k) * (Dh ** -0.5)
        p = _softmax(scores, axis=-1)
        o = torch.einsum("bhqk,bhkd->bhqd", p, v)
        return (lambda x, *d: x.permute(*d))(o, 0, 2, 1, 3).reshape(B, Sq, Hq * Dh) @ wo

    def _B_ref(x, k_cache, v_cache, wq, wo):
        B, Sq, D = x.shape
        Hq, Dh = 32, 128
        Hkv = k_cache.shape[1]
        rep = Hq // Hkv
        q = (lambda x, *d: x.permute(*d))((x @ wq).reshape(B, Sq, Hq, Dh), 0, 2, 1, 3)
        k = (lambda x, n, axis: torch.repeat_interleave(x, n, dim=axis))(k_cache, rep, axis=1)
        v = (lambda x, n, axis: torch.repeat_interleave(x, n, dim=axis))(v_cache, rep, axis=1)
        scores = torch.einsum("bhqd,bhkd->bhqk", q, k) * (Dh ** -0.5)
        p = _softmax(scores, axis=-1)
        o = torch.einsum("bhqk,bhkd->bhqd", p, v)
        return (lambda x, *d: x.permute(*d))(o, 0, 2, 1, 3).reshape(B, Sq, Hq * Dh) @ wo

    a = _A_ref(A_x, A_k_cache, A_v_cache, A_wq, A_wo)
    b = _B_ref(B_x, B_k_cache, B_v_cache, B_wq, B_wo)
    return torch.concatenate([a, b], axis=-1)
```

```cpp
#include <hip/hip_runtime.h>
#include <hip/hip_bf16.h>
#include <cstdint>

#define GAS __attribute__((address_space(1)))
#define LAS __attribute__((address_space(3)))
typedef unsigned v4u __attribute__((ext_vector_type(4)));
typedef unsigned v2u __attribute__((ext_vector_type(2)));
typedef float f32x4 __attribute__((ext_vector_type(4)));
typedef short bf16x8 __attribute__((ext_vector_type(8)));
typedef unsigned short u16;

constexpr int DM = 1024, HQ = 32, HKV = 4, SK = 32768, DH = 128, NB = 2, G = 8;
constexpr int NSPLIT = 16, KEYS_PER_WG = SK / NSPLIT;
constexpr float C2 = 0.088388347648318440f * 1.4426950408889634f;

constexpr size_t WS_Q = 0;
constexpr size_t WS_PML = 65536;
constexpr size_t WS_PO = WS_PML + 2 * 2 * 32 * 16 * 2 * 4;
constexpr size_t WS_OP = WS_PO + (size_t)2 * 2 * 32 * 16 * 128 * 4;

struct Ptrs { const u16* x[2]; const u16* k[2]; const u16* v[2]; const u16* wq[2]; const u16* wo[2]; u16* out; unsigned char* ws; };

__device__ __forceinline__ float bflo(unsigned u) { return __builtin_bit_cast(float, u << 16); }
__device__ __forceinline__ float bfhi(unsigned u) { return __builtin_bit_cast(float, u & 0xffff0000u); }
__device__ __forceinline__ unsigned cvtpk(float lo, float hi) {
  unsigned r; asm("v_cvt_pk_bf16_f32 %0, %1, %2" : "=v"(r) : "v"(lo), "v"(hi)); return r;
}
__device__ __forceinline__ f32x4 mfma(v4u a, v4u b, f32x4 c) {
  return __builtin_amdgcn_mfma_f32_16x16x32_bf16(__builtin_bit_cast(bf16x8, a), __builtin_bit_cast(bf16x8, b), c, 0, 0, 0);
}
__device__ __forceinline__ float xrow_max(float x) {
  auto s = __builtin_amdgcn_permlane16_swap(__float_as_uint(x), __float_as_uint(x), false, false);
  x = fmaxf(__uint_as_float(s[0]), __uint_as_float(s[1]));
  auto t = __builtin_amdgcn_permlane32_swap(__float_as_uint(x), __float_as_uint(x), false, false);
  return fmaxf(__uint_as_float(t[0]), __uint_as_float(t[1]));
}
__device__ __forceinline__ float xrow_sum(float x) {
  auto s = __builtin_amdgcn_permlane16_swap(__float_as_uint(x), __float_as_uint(x), false, false);
  x = __uint_as_float(s[0]) + __uint_as_float(s[1]);
  auto t = __builtin_amdgcn_permlane32_swap(__float_as_uint(x), __float_as_uint(x), false, false);
  return __uint_as_float(t[0]) + __uint_as_float(t[1]);
}
__device__ __forceinline__ v4u ldnt(const u16* p) { return __builtin_nontemporal_load((const GAS v4u*)p); }

__global__ void __launch_bounds__(256) k_qproj(Ptrs p) {
  __shared__ float red[64][2][32 + 1];
  const int br = blockIdx.x >> 7, cg = blockIdx.x & 127, tid = threadIdx.x;
  const int cq = tid & 3, rg = tid >> 2;
  const u16* wq = p.wq[br] + cg * 32 + cq * 8;
  const u16* x = p.x[br];
  v4u w[16];
#pragma unroll
  for (int i = 0; i < 16; ++i) w[i] = ldnt(wq + (size_t)(rg + 64 * i) * 4096);
  float a0[8] = {}, a1[8] = {};
#pragma unroll
  for (int i = 0; i < 16; ++i) {
    const int r = rg + 64 * i;
    const float x0 = bflo(x[r]), x1 = bflo(x[DM + r]);
#pragma unroll
    for (int j = 0; j < 4; ++j) {
      a0[2 * j] = fmaf(x0, bflo(w[i][j]), a0[2 * j]); a0[2 * j + 1] = fmaf(x0, bfhi(w[i][j]), a0[2 * j + 1]);
      a1[2 * j] = fmaf(x1, bflo(w[i][j]), a1[2 * j]); a1[2 * j + 1] = fmaf(x1, bfhi(w[i][j]), a1[2 * j + 1]);
    }
  }
#pragma unroll
  for (int j = 0; j < 8; ++j) { red[rg][0][cq * 8 + j] = a0[j]; red[rg][1][cq * 8 + j] = a1[j]; }
  __syncthreads();
  if (tid < 64) {
    const int b = tid >> 5, c = tid & 31;
    float s = 0.f;
    for (int r = 0; r < 64; ++r) s += red[r][b][c];
    u16* q = (u16*)(p.ws + WS_Q);
    q[(br * 2 + b) * 4096 + cg * 32 + c] = (u16)(cvtpk(s, s) & 0xffffu);
  }
}

constexpr int AW = 8;
__global__ void __launch_bounds__(AW * 64, 2) k_attn(Ptrs p) {
  __shared__ __attribute__((aligned(16))) float mrg[AW][G][DH + 4];
  const int bid = blockIdx.x;
  const int split = bid & 15, kvh = (bid >> 4) & 3, b = (bid >> 6) & 1, br = bid >> 7;
  const int tid = threadIdx.x, lane = tid & 63, wave = __builtin_amdgcn_readfirstlane(tid >> 6);
  const int c = lane & 15, g = lane >> 4;
  const size_t base = ((size_t)(b * HKV + kvh) * SK + (size_t)split * KEYS_PER_WG) * DH;
  const u16* kp = p.k[br] + base + (size_t)(4 * (c & 3) + (c >> 2)) * DH + 8 * g;
  const u16* vp = p.v[br] + base + (size_t)g * DH + 8 * c;
  v4u qf[4];
  {
    const u16* q = (const u16*)(p.ws + WS_Q) + (br * 2 + b) * 4096 + (kvh * G + (c & 7)) * DH + 8 * g;
#pragma unroll
    for (int s = 0; s < 4; ++s) { v4u t = *(const GAS v4u*)(q + 32 * s); qf[s] = c < 8 ? t : v4u{0u, 0u, 0u, 0u}; }
  }
  constexpr int NT = KEYS_PER_WG / 32 / AW;
  v4u K[2][8], V[2][8];
  auto issue = [&](int i, v4u (&Kr)[8], v4u (&Vr)[8]) {
    const size_t off = (size_t)(wave + AW * i) * 32 * DH;
#pragma unroll
    for (int t = 0; t < 2; ++t)
#pragma unroll
      for (int s = 0; s < 4; ++s) Kr[t * 4 + s] = ldnt(kp + off + t * 16 * DH + 32 * s);
#pragma unroll
    for (int j = 0; j < 8; ++j) Vr[j] = ldnt(vp + off + 4 * j * DH);
  };
  f32x4 acc[8];
#pragma unroll
  for (int e = 0; e < 8; ++e) acc[e] = f32x4{0.f, 0.f, 0.f, 0.f};
  float m = -INFINITY, l = 0.f;
  auto compute = [&](v4u (&Kr)[8], v4u (&Vr)[8]) {
    f32x4 s0 = {0.f, 0.f, 0.f, 0.f}, s1 = {0.f, 0.f, 0.f, 0.f};
#pragma unroll
    for (int s = 0; s < 4; ++s) { s0 = mfma(Kr[s], qf[s], s0); s1 = mfma(Kr[4 + s], qf[s], s1); }
    float tm = fmaxf(fmaxf(fmaxf(s0[0], s0[1]), fmaxf(s0[2], s0[3])), fmaxf(fmaxf(s1[0], s1[1]), fmaxf(s1[2], s1[3])));
    tm = xrow_max(tm) * C2;
    const float mn = fmaxf(m, tm);
    const float alpha = __builtin_amdgcn_exp2f(m - mn);
    m = mn;
    float pr[8];
#pragma unroll
    for (int i = 0; i < 4; ++i) { pr[i] = __builtin_amdgcn_exp2f(fmaf(s0[i], C2, -mn)); pr[4 + i] = __builtin_amdgcn_exp2f(fmaf(s1[i], C2, -mn)); }
    l = l * alpha + (((pr[0] + pr[1]) + (pr[2] + pr[3])) + ((pr[4] + pr[5]) + (pr[6] + pr[7])));
    const v4u pf = {cvtpk(pr[0], pr[1]), cvtpk(pr[2], pr[3]), cvtpk(pr[4], pr[5]), cvtpk(pr[6], pr[7])};
#pragma unroll
    for (int e = 0; e < 8; ++e) {
      v4u a;
#pragma unroll
      for (int jj = 0; jj < 4; ++jj) a[jj] = __builtin_amdgcn_perm(Vr[2 * jj + 1][e >> 1], Vr[2 * jj][e >> 1], (e & 1) ? 0x07060302u : 0x05040100u);
      acc[e] = mfma(a, pf, acc[e] * alpha);
    }
  };
  issue(0, K[0], V[0]);
#pragma unroll 1
  for (int i = 0; i < NT; i += 2) {
    issue(i + 1, K[1], V[1]);
    __builtin_amdgcn_sched_barrier(0);
    compute(K[0], V[0]);
    __builtin_amdgcn_sched_barrier(0);
    if (i + 2 < NT) issue(i + 2, K[0], V[0]);
    __builtin_amdgcn_sched_barrier(0);
    compute(K[1], V[1]);
    __builtin_amdgcn_sched_barrier(0);
  }
  l = xrow_sum(l);
  if (c < 8) {
#pragma unroll
    for (int r = 0; r < 4; ++r) {
      const int d0 = 8 * (4 * g + r);
      *(f32x4*)&mrg[wave][c][4 + d0] = f32x4{acc[0][r], acc[1][r], acc[2][r], acc[3][r]};
      *(f32x4*)&mrg[wave][c][4 + d0 + 4] = f32x4{acc[4][r], acc[5][r], acc[6][r], acc[7][r]};
    }
    if (g == 0) { mrg[wave][c][0] = m; mrg[wave][c][1] = l; }
  }
  __syncthreads();
  {
    const int h = wave, d0 = 2 * lane;
    float M = -INFINITY;
#pragma unroll
    for (int w = 0; w < AW; ++w) M = fmaxf(M, mrg[w][h][0]);
    float L = 0.f, o0 = 0.f, o1 = 0.f;
#pragma unroll
    for (int w = 0; w < AW; ++w) {
      const float wt = __builtin_amdgcn_exp2f(mrg[w][h][0] - M);
      L = fmaf(wt, mrg[w][h][1], L); o0 = fmaf(wt, mrg[w][h][4 + d0], o0); o1 = fmaf(wt, mrg[w][h][4 + d0 + 1], o1);
    }
    const size_t slot = ((size_t)((br * 2 + b) * HQ + kvh * G + h)) * NSPLIT + split;
    float* po = (float*)(p.ws + WS_PO); float* pml = (float*)(p.ws + WS_PML);
    *(float2*)(po + slot * DH + d0) = make_float2(o0, o1);
    if (lane == 0) *(float2*)(pml + slot * 2) = make_float2(M, L);
  }
}

__global__ void __launch_bounds__(256) k_oproj(Ptrs p) {
  __shared__ float o[2][32];
  __shared__ float red[2][2][1024];
  const int br = blockIdx.x >> 7, rgp = blockIdx.x & 127, tid = threadIdx.x;
  const int h = rgp >> 2, dq = rgp & 3;
  const int half = tid >> 7, cc = tid & 127;
  const u16* wo = p.wo[br] + (size_t)(rgp * 32 + half * 16) * 1024 + cc * 8;
  v4u w[16];
#pragma unroll
  for (int i = 0; i < 16; ++i) w[i] = ldnt(wo + (size_t)i * 1024);
  if (tid < 64) {
    const int b = tid >> 5, d = tid & 31;
    const float* pml = (const float*)(p.ws + WS_PML) + (size_t)((br * 2 + b) * HQ + h) * NSPLIT * 2;
    const float* po = (const float*)(p.ws + WS_PO) + (size_t)((br * 2 + b) * HQ + h) * NSPLIT * DH + dq * 32 + d;
    float M = -INFINITY;
#pragma unroll
    for (int s = 0; s < NSPLIT; ++s) M = fmaxf(M, pml[2 * s]);
    float L = 0.f, a = 0.f;
#pragma unroll
    for (int s = 0; s < NSPLIT; ++s) { const float wt = __builtin_amdgcn_exp2f(pml[2 * s] - M); L = fmaf(wt, pml[2 * s + 1], L); a = fmaf(wt, po[s * DH], a); }
    const float ov = a / L;
    o[b][d] = bflo(cvtpk(ov, ov) & 0xffffu);
  }
  __syncthreads();
  float a0[8] = {}, a1[8] = {};
#pragma unroll
  for (int i = 0; i < 16; ++i) {
    const float x0 = o[0][half * 16 + i], x1 = o[1][half * 16 + i];
#pragma unroll
    for (int j = 0; j < 4; ++j) {
      a0[2 * j] = fmaf(x0, bflo(w[i][j]), a0[2 * j]); a0[2 * j + 1] = fmaf(x0, bfhi(w[i][j]), a0[2 * j + 1]);
      a1[2 * j] = fmaf(x1, bflo(w[i][j]), a1[2 * j]); a1[2 * j + 1] = fmaf(x1, bfhi(w[i][j]), a1[2 * j + 1]);
    }
  }
#pragma unroll
  for (int j = 0; j < 8; ++j) { red[half][0][cc * 8 + j] = a0[j]; red[half][1][cc * 8 + j] = a1[j]; }
  __syncthreads();
  float* op = (float*)(p.ws + WS_OP) + (size_t)blockIdx.x * 2048;
#pragma unroll
  for (int i = 0; i < 8; ++i) { const int e = tid + 256 * i; op[e] = red[0][e >> 10][e & 1023] + red[1][e >> 10][e & 1023]; }
}

__global__ void __launch_bounds__(256) k_reduce(Ptrs p) {
  __shared__ float red[16][17];
  const int br = blockIdx.x >> 7, og = blockIdx.x & 127, tid = threadIdx.x;
  const int e = og * 16 + (tid & 15), pg = tid >> 4;
  const float* op = (const float*)(p.ws + WS_OP) + (size_t)br * 128 * 2048 + e;
  float s = 0.f;
#pragma unroll
  for (int i = 0; i < 8; ++i) s += op[(size_t)(pg * 8 + i) * 2048];
  red[pg][tid & 15] = s;
  __syncthreads();
  if (tid < 16) {
    float t = 0.f;
#pragma unroll
    for (int i = 0; i < 16; ++i) t += red[i][tid];
    const int b = e >> 10, n = e & 1023;
    p.out[b * 2048 + br * 1024 + n] = (u16)(cvtpk(t, t) & 0xffffu);
  }
}

extern "C" void kernel_launch(void* const* d_in, const int* in_sizes, int n_in, void* d_out, int out_size, void* d_ws, size_t ws_size,
                              hipStream_t stream) {
  Ptrs p;
  for (int br = 0; br < 2; ++br) {
    p.x[br] = (const u16*)d_in[5 * br + 0]; p.k[br] = (const u16*)d_in[5 * br + 1]; p.v[br] = (const u16*)d_in[5 * br + 2];
    p.wq[br] = (const u16*)d_in[5 * br + 3]; p.wo[br] = (const u16*)d_in[5 * br + 4];
  }
  p.out = (u16*)d_out; p.ws = (unsigned char*)d_ws;
  k_qproj<<<256, 256, 0, stream>>>(p);
  k_attn<<<256, AW * 64, 0, stream>>>(p);
  k_oproj<<<256, 256, 0, stream>>>(p);
  k_reduce<<<256, 256, 0, stream>>>(p);
}
```

```cpp
#define STAT 1
#include <hip/hip_runtime.h>
#include <hip/hip_bf16.h>
#include <cstdint>

#define GAS __attribute__((address_space(1)))
#define LAS __attribute__((address_space(3)))
typedef unsigned v4u __attribute__((ext_vector_type(4)));
typedef unsigned v2u __attribute__((ext_vector_type(2)));
typedef float f32x4 __attribute__((ext_vector_type(4)));
typedef short bf16x8 __attribute__((ext_vector_type(8)));
typedef unsigned short u16;

constexpr int DM = 1024, HQ = 32, HKV = 4, SK = 32768, DH = 128, NB = 2, G = 8;
constexpr int NSPLIT = 16, KEYS_PER_WG = SK / NSPLIT;
constexpr float C2 = 0.088388347648318440f * 1.4426950408889634f;

constexpr size_t WS_ST = 32768;
constexpr size_t WS_Q = 0;
constexpr size_t WS_PML = 65536;
constexpr size_t WS_PO = WS_PML + 2 * 2 * 32 * 16 * 2 * 4;
constexpr size_t WS_OP = WS_PO + (size_t)2 * 2 * 32 * 16 * 128 * 4;

struct Ptrs { const u16* x[2]; const u16* k[2]; const u16* v[2]; const u16* wq[2]; const u16* wo[2]; u16* out; unsigned char* ws; };

__device__ __forceinline__ float bflo(unsigned u) { return __builtin_bit_cast(float, u << 16); }
__device__ __forceinline__ float bfhi(unsigned u) { return __builtin_bit_cast(float, u & 0xffff0000u); }
__device__ __forceinline__ unsigned cvtpk(float lo, float hi) {
  unsigned r; asm("v_cvt_pk_bf16_f32 %0, %1, %2" : "=v"(r) : "v"(lo), "v"(hi)); return r;
}
__device__ __forceinline__ f32x4 mfma(v4u a, v4u b, f32x4 c) {
  return __builtin_amdgcn_mfma_f32_16x16x32_bf16(__builtin_bit_cast(bf16x8, a), __builtin_bit_cast(bf16x8, b), c, 0, 0, 0);
}
__device__ __forceinline__ float xrow_max(float x) {
  auto s = __builtin_amdgcn_permlane16_swap(__float_as_uint(x), __float_as_uint(x), false, false);
  x = fmaxf(__uint_as_float(s[0]), __uint_as_float(s[1]));
  auto t = __builtin_amdgcn_permlane32_swap(__float_as_uint(x), __float_as_uint(x), false, false);
  return fmaxf(__uint_as_float(t[0]), __uint_as_float(t[1]));
}
__device__ __forceinline__ float xrow_sum(float x) {
  auto s = __builtin_amdgcn_permlane16_swap(__float_as_uint(x), __float_as_uint(x), false, false);
  x = __uint_as_float(s[0]) + __uint_as_float(s[1]);
  auto t = __builtin_amdgcn_permlane32_swap(__float_as_uint(x), __float_as_uint(x), false, false);
  return __uint_as_float(t[0]) + __uint_as_float(t[1]);
}
__device__ __forceinline__ v4u ldnt(const u16* p) { return __builtin_nontemporal_load((const GAS v4u*)p); }

__global__ void __launch_bounds__(256) k_qproj(Ptrs p) {
  __shared__ float red[64][2][32 + 1];
  const int br = blockIdx.x >> 7, cg = blockIdx.x & 127, tid = threadIdx.x;
  const int cq = tid & 3, rg = tid >> 2;
  const u16* wq = p.wq[br] + cg * 32 + cq * 8;
  const u16* x = p.x[br];
  v4u w[16];
#pragma unroll
  for (int i = 0; i < 16; ++i) w[i] = ldnt(wq + (size_t)(rg + 64 * i) * 4096);
  float a0[8] = {}, a1[8] = {};
#pragma unroll
  for (int i = 0; i < 16; ++i) {
    const int r = rg + 64 * i;
    const float x0 = bflo(x[r]), x1 = bflo(x[DM + r]);
#pragma unroll
    for (int j = 0; j < 4; ++j) {
      a0[2 * j] = fmaf(x0, bflo(w[i][j]), a0[2 * j]); a0[2 * j + 1] = fmaf(x0, bfhi(w[i][j]), a0[2 * j + 1]);
      a1[2 * j] = fmaf(x1, bflo(w[i][j]), a1[2 * j]); a1[2 * j + 1] = fmaf(x1, bfhi(w[i][j]), a1[2 * j + 1]);
    }
  }
#pragma unroll
  for (int j = 0; j < 8; ++j) { red[rg][0][cq * 8 + j] = a0[j]; red[rg][1][cq * 8 + j] = a1[j]; }
  __syncthreads();
  if (tid < 64) {
    const int b = tid >> 5, c = tid & 31;
    float s = 0.f;
    for (int r = 0; r < 64; ++r) s += red[r][b][c];
    u16* q = (u16*)(p.ws + WS_Q);
    q[(br * 2 + b) * 4096 + cg * 32 + c] = (u16)(cvtpk(s, s) & 0xffffu);
  }
}

constexpr int AW = 8;
__global__ void __launch_bounds__(AW * 64, 2) k_attn(Ptrs p) {
  __shared__ __attribute__((aligned(16))) float mrg[AW][G][DH + 4];
  const unsigned long long t0 = __builtin_amdgcn_s_memrealtime();
  const int bid = blockIdx.x;
  const int split = bid & 15, kvh = (bid >> 4) & 3, b = (bid >> 6) & 1, br = bid >> 7;
  const int tid = threadIdx.x, lane = tid & 63, wave = __builtin_amdgcn_readfirstlane(tid >> 6);
  const int c = lane & 15, g = lane >> 4;
  const size_t base = ((size_t)(b * HKV + kvh) * SK + (size_t)split * KEYS_PER_WG) * DH;
  const u16* kp = p.k[br] + base + (size_t)(4 * (c & 3) + (c >> 2)) * DH + 8 * g;
  const u16* vp = p.v[br] + base + (size_t)g * DH + 8 * c;
  v4u qf[4];
  {
    const u16* q = (const u16*)(p.ws + WS_Q) + (br * 2 + b) * 4096 + (kvh * G + (c & 7)) * DH + 8 * g;
#pragma unroll
    for (int s = 0; s < 4; ++s) { v4u t = *(const GAS v4u*)(q + 32 * s); qf[s] = c < 8 ? t : v4u{0u, 0u, 0u, 0u}; }
  }
  constexpr int NT = KEYS_PER_WG / 32 / AW;
  v4u K[2][8], V[2][8];
  auto issue = [&](int i, v4u (&Kr)[8], v4u (&Vr)[8]) {
    const size_t off = (size_t)(wave + AW * i) * 32 * DH;
#pragma unroll
    for (int t = 0; t < 2; ++t)
#pragma unroll
      for (int s = 0; s < 4; ++s) Kr[t * 4 + s] = ldnt(kp + off + t * 16 * DH + 32 * s);
#pragma unroll
    for (int j = 0; j < 8; ++j) Vr[j] = ldnt(vp + off + 4 * j * DH);
  };
  f32x4 acc[8];
#pragma unroll
  for (int e = 0; e < 8; ++e) acc[e] = f32x4{0.f, 0.f, 0.f, 0.f};
  float m = -INFINITY, l = 0.f;
  auto compute = [&](v4u (&Kr)[8], v4u (&Vr)[8]) {
    f32x4 s0 = {0.f, 0.f, 0.f, 0.f}, s1 = {0.f, 0.f, 0.f, 0.f};
#pragma unroll
    for (int s = 0; s < 4; ++s) { s0 = mfma(Kr[s], qf[s], s0); s1 = mfma(Kr[4 + s], qf[s], s1); }
    float tm = fmaxf(fmaxf(fmaxf(s0[0], s0[1]), fmaxf(s0[2], s0[3])), fmaxf(fmaxf(s1[0], s1[1]), fmaxf(s1[2], s1[3])));
    tm = xrow_max(tm) * C2;
    const float mn = fmaxf(m, tm);
    const float alpha = __builtin_amdgcn_exp2f(m - mn);
    m = mn;
    float pr[8];
#pragma unroll
    for (int i = 0; i < 4; ++i) { pr[i] = __builtin_amdgcn_exp2f(fmaf(s0[i], C2, -mn)); pr[4 + i] = __builtin_amdgcn_exp2f(fmaf(s1[i], C2, -mn)); }
    l = l * alpha + (((pr[0] + pr[1]) + (pr[2] + pr[3])) + ((pr[4] + pr[5]) + (pr[6] + pr[7])));
    const v4u pf = {cvtpk(pr[0], pr[1]), cvtpk(pr[2], pr[3]), cvtpk(pr[4], pr[5]), cvtpk(pr[6], pr[7])};
#pragma unroll
    for (int e = 0; e < 8; ++e) {
      v4u a;
#pragma unroll
      for (int jj = 0; jj < 4; ++jj) a[jj] = __builtin_amdgcn_perm(Vr[2 * jj + 1][e >> 1], Vr[2 * jj][e >> 1], (e & 1) ? 0x07060302u : 0x05040100u);
      acc[e] = mfma(a, pf, acc[e] * alpha);
    }
  };
  issue(0, K[0], V[0]);
#pragma unroll 1
  for (int i = 0; i < NT; i += 2) {
    issue(i + 1, K[1], V[1]);
    __builtin_amdgcn_sched_barrier(0);
    compute(K[0], V[0]);
    __builtin_amdgcn_sched_barrier(0);
    if (i + 2 < NT) issue(i + 2, K[0], V[0]);
    __builtin_amdgcn_sched_barrier(0);
    compute(K[1], V[1]);
    __builtin_amdgcn_sched_barrier(0);
  }
  l = xrow_sum(l);
  if (c < 8) {
#pragma unroll
    for (int r = 0; r < 4; ++r) {
      const int d0 = 8 * (4 * g + r);
      *(f32x4*)&mrg[wave][c][4 + d0] = f32x4{acc[0][r], acc[1][r], acc[2][r], acc[3][r]};
      *(f32x4*)&mrg[wave][c][4 + d0 + 4] = f32x4{acc[4][r], acc[5][r], acc[6][r], acc[7][r]};
    }
    if (g == 0) { mrg[wave][c][0] = m; mrg[wave][c][1] = l; }
  }
  __syncthreads();
  {
    const int h = wave, d0 = 2 * lane;
    float M = -INFINITY;
#pragma unroll
    for (int w = 0; w < AW; ++w) M = fmaxf(M, mrg[w][h][0]);
    float L = 0.f, o0 = 0.f, o1 = 0.f;
#pragma unroll
    for (int w = 0; w < AW; ++w) {
      const float wt = __builtin_amdgcn_exp2f(mrg[w][h][0] - M);
      L = fmaf(wt, mrg[w][h][1], L); o0 = fmaf(wt, mrg[w][h][4 + d0], o0); o1 = fmaf(wt, mrg[w][h][4 + d0 + 1], o1);
    }
    const size_t slot = ((size_t)((br * 2 + b) * HQ + kvh * G + h)) * NSPLIT + split;
    float* po = (float*)(p.ws + WS_PO); float* pml = (float*)(p.ws + WS_PML);
    *(float2*)(po + slot * DH + d0) = make_float2(o0, o1);
    if (lane == 0) *(float2*)(pml + slot * 2) = make_float2(M, L);
    if (tid == 0) { unsigned long long* st = (unsigned long long*)(p.ws + WS_ST); st[2 * bid] = t0; st[2 * bid + 1] = __builtin_amdgcn_s_memrealtime(); }
  }
}

__global__ void __launch_bounds__(256) k_oproj(Ptrs p) {
  __shared__ float o[2][32];
  __shared__ float red[2][2][1024];
  const int br = blockIdx.x >> 7, rgp = blockIdx.x & 127, tid = threadIdx.x;
  const int h = rgp >> 2, dq = rgp & 3;
  const int half = tid >> 7, cc = tid & 127;
  const u16* wo = p.wo[br] + (size_t)(rgp * 32 + half * 16) * 1024 + cc * 8;
  v4u w[16];
#pragma unroll
  for (int i = 0; i < 16; ++i) w[i] = ldnt(wo + (size_t)i * 1024);
  if (tid < 64) {
    const int b = tid >> 5, d = tid & 31;
    const float* pml = (const float*)(p.ws + WS_PML) + (size_t)((br * 2 + b) * HQ + h) * NSPLIT * 2;
    const float* po = (const float*)(p.ws + WS_PO) + (size_t)((br * 2 + b) * HQ + h) * NSPLIT * DH + dq * 32 + d;
    float M = -INFINITY;
#pragma unroll
    for (int s = 0; s < NSPLIT; ++s) M = fmaxf(M, pml[2 * s]);
    float L = 0.f, a = 0.f;
#pragma unroll
    for (int s = 0; s < NSPLIT; ++s) { const float wt = __builtin_amdgcn_exp2f(pml[2 * s] - M); L = fmaf(wt, pml[2 * s + 1], L); a = fmaf(wt, po[s * DH], a); }
    const float ov = a / L;
    o[b][d] = bflo(cvtpk(ov, ov) & 0xffffu);
  }
  __syncthreads();
  float a0[8] = {}, a1[8] = {};
#pragma unroll
  for (int i = 0; i < 16; ++i) {
    const float x0 = o[0][half * 16 + i], x1 = o[1][half * 16 + i];
#pragma unroll
    for (int j = 0; j < 4; ++j) {
      a0[2 * j] = fmaf(x0, bflo(w[i][j]), a0[2 * j]); a0[2 * j + 1] = fmaf(x0, bfhi(w[i][j]), a0[2 * j + 1]);
      a1[2 * j] = fmaf(x1, bflo(w[i][j]), a1[2 * j]); a1[2 * j + 1] = fmaf(x1, bfhi(w[i][j]), a1[2 * j + 1]);
    }
  }
#pragma unroll
  for (int j = 0; j < 8; ++j) { red[half][0][cc * 8 + j] = a0[j]; red[half][1][cc * 8 + j] = a1[j]; }
  __syncthreads();
  float* op = (float*)(p.ws + WS_OP) + (size_t)blockIdx.x * 2048;
#pragma unroll
  for (int i = 0; i < 8; ++i) { const int e = tid + 256 * i; op[e] = red[0][e >> 10][e & 1023] + red[1][e >> 10][e & 1023]; }
}

__global__ void __launch_bounds__(256) k_reduce(Ptrs p) {
  __shared__ float red[16][17];
  const int br = blockIdx.x >> 7, og = blockIdx.x & 127, tid = threadIdx.x;
  const int e = og * 16 + (tid & 15), pg = tid >> 4;
  const float* op = (const float*)(p.ws + WS_OP) + (size_t)br * 128 * 2048 + e;
  float s = 0.f;
#pragma unroll
  for (int i = 0; i < 8; ++i) s += op[(size_t)(pg * 8 + i) * 2048];
  red[pg][tid & 15] = s;
  __syncthreads();
  if (tid < 16) {
    float t = 0.f;
#pragma unroll
    for (int i = 0; i < 16; ++i) t += red[i][tid];
    const int b = e >> 10, n = e & 1023;
    p.out[b * 2048 + br * 1024 + n] = (u16)(cvtpk(t, t) & 0xffffu);
  }
  if (blockIdx.x == 0 && tid == 0 && STAT > 0) {
    const unsigned long long* st = (const unsigned long long*)(p.ws + WS_ST);
    unsigned long long s0 = ~0ull, e1 = 0, e0 = ~0ull, sa = 0, sb = 0;
    for (int i = 0; i < 256; ++i) { s0 = st[2 * i] < s0 ? st[2 * i] : s0; e1 = st[2 * i + 1] > e1 ? st[2 * i + 1] : e1; e0 = st[2 * i + 1] < e0 ? st[2 * i + 1] : e0; }
    for (int i = 0; i < 128; ++i) { sa += st[2 * i + 1] - s0; sb += st[2 * (i + 128) + 1] - s0; }
    unsigned long long stat = STAT == 1 ? 0 : STAT == 2 ? e1 - s0 : STAT == 3 ? sa / 128 : STAT == 4 ? sb / 128 : e0 - s0;
    if (stat > 20000) stat = 20000;
    const unsigned long long tb = __builtin_amdgcn_s_memrealtime();
    while (__builtin_amdgcn_s_memrealtime() - tb < 4 * stat) __builtin_amdgcn_s_sleep(1);
  }
}

extern "C" void kernel_launch(void* const* d_in, const int* in_sizes, int n_in, void* d_out, int out_size, void* d_ws, size_t ws_size,
                              hipStream_t stream) {
  Ptrs p;
  for (int br = 0; br < 2; ++br) {
    p.x[br] = (const u16*)d_in[5 * br + 0]; p.k[br] = (const u16*)d_in[5 * br + 1]; p.v[br] = (const u16*)d_in[5 * br + 2];
    p.wq[br] = (const u16*)d_in[5 * br + 3]; p.wo[br] = (const u16*)d_in[5 * br + 4];
  }
  p.out = (u16*)d_out; p.ws = (unsigned char*)d_ws;
  k_qproj<<<256, 256, 0, stream>>>(p);
  k_attn<<<256, AW * 64, 0, stream>>>(p);
  k_oproj<<<256, 256, 0, stream>>>(p);
  k_reduce<<<256, 256, 0, stream>>>(p);
}
```

```cpp
#include <hip/hip_runtime.h>
#include <hip/hip_bf16.h>
#include <cstdint>

#define GAS __attribute__((address_space(1)))
#define LAS __attribute__((address_space(3)))
typedef unsigned v4u __attribute__((ext_vector_type(4)));
typedef float f32x4 __attribute__((ext_vector_type(4)));
typedef short bf16x8 __attribute__((ext_vector_type(8)));
typedef short s16x4 __attribute__((ext_vector_type(4)));
typedef unsigned v2u __attribute__((ext_vector_type(2)));
typedef unsigned short u16;
typedef unsigned long long u64;

#define CFG_WIN 24
#define CFG_DYN 0
#define CFG_NSTATIC 48
#define CFG_WOLAST 1
#define CFG_STAMPS 0

#define SWZ 15
namespace fused {

constexpr int SK = 32768, DH = 128;
constexpr int NWG = 256, NCONS = 4, NTHR = 64 * (NCONS + 2);
constexpr int NSLOT = 8, SLOT = 16384;
constexpr int TILES = SK / 32;
constexpr float C2 = 0.088388347648318440f * 1.4426950408889634f;
constexpr u64 TCAP = 2000000;

constexpr int WS_EPOCH = 0;
constexpr int WS_TICKET = 4096;
constexpr int WS_ML = 16384;
constexpr int WS_Q = 65536;
constexpr int WS_H2 = WS_Q + 8 * 2 * 4096;
constexpr int WS_H1 = WS_H2 + 256 * 4096;
constexpr int WS_END = WS_H1 + 256 * 32 * 256;
constexpr int WS_STAMP = WS_END;

constexpr int L_RING = 0;
constexpr int L_CTL = NSLOT * SLOT;
constexpr int L_QIMG = L_CTL + 256;
constexpr int L_QRED = L_QIMG + 2048;
constexpr int L_OFRAG = L_QRED + 1024;
constexpr int MROW = 528;
constexpr int L_MRG = L_OFRAG + 4096;
constexpr int L_MML = L_MRG + 4 * 8 * MROW;
constexpr int L_BYTES = L_MML + 256;
enum { W_FULL = 0, W_FREE = 16, W_ABORT = 32, W_END = 33, W_QCNT = 34, W_QRDY = 35, W_MCNT = 36, W_ORDY = 37, W_PROG = 38, W_TKT = 40, W_TAGA = 48, W_TAGB = 49, W_QPUB = 50 };
static_assert(L_BYTES <= 160 * 1024 && L_BYTES > 80 * 1024, "one workgroup per CU");

struct Params { const u16* x[2]; const u16* k[2]; const u16* v[2]; const u16* wq[2]; const u16* wo[2]; u16* out; unsigned char* ws; };

__device__ __forceinline__ u64 rt() { return __builtin_amdgcn_s_memrealtime(); }
__device__ __forceinline__ unsigned rfl(unsigned v) { return (unsigned)__builtin_amdgcn_readfirstlane((int)v); }
__device__ __forceinline__ unsigned cvtpk(float lo, float hi) {
  unsigned r; asm("v_cvt_pk_bf16_f32 %0, %1, %2" : "=v"(r) : "v"(lo), "v"(hi)); return r;
}
__device__ __forceinline__ f32x4 mfma(v4u a, v4u b, f32x4 c) {
  return __builtin_amdgcn_mfma_f32_16x16x32_bf16(__builtin_bit_cast(bf16x8, a), __builtin_bit_cast(bf16x8, b), c, 0, 0, 0);
}
__device__ __forceinline__ float f(unsigned u) { return __uint_as_float(u); }
__device__ __forceinline__ unsigned u(float x) { return __float_as_uint(x); }
__device__ __forceinline__ float xrow_max(float x) {
  auto s = __builtin_amdgcn_permlane16_swap(u(x), u(x), false, false);
  x = fmaxf(f(s[0]), f(s[1]));
  auto t = __builtin_amdgcn_permlane32_swap(u(x), u(x), false, false);
  return fmaxf(f(t[0]), f(t[1]));
}
__device__ __forceinline__ float xrow_sum(float x) {
  auto s = __builtin_amdgcn_permlane16_swap(u(x), u(x), false, false);
  x = f(s[0]) + f(s[1]);
  auto t = __builtin_amdgcn_permlane32_swap(u(x), u(x), false, false);
  return f(t[0]) + f(t[1]);
}
template <int CTRL> __device__ __forceinline__ float dpp(float x) {
  return __builtin_bit_cast(float, __builtin_amdgcn_mov_dpp(__builtin_bit_cast(int, x), CTRL, 0xf, 0xf, true));
}
constexpr int ROR2 = 0x122, ROR4 = 0x124, ROR8 = 0x128;
template <int N> __device__ __forceinline__ void vmwait() { asm volatile("s_waitcnt vmcnt(%0)" ::"i"(N) : "memory"); }
#define LDS_WAIT() asm volatile("s_waitcnt lgkmcnt(0)" ::: "memory")
#define PIN8(w) asm volatile("" : "+v"(w[0]), "+v"(w[1]), "+v"(w[2]), "+v"(w[3]), "+v"(w[4]), "+v"(w[5]), "+v"(w[6]), "+v"(w[7]) : : "memory")

__device__ __forceinline__ v4u tfrag(const v4u (&r)[8], int e) {
  v4u a;
#pragma unroll
  for (int jj = 0; jj < 4; ++jj) a[jj] = __builtin_amdgcn_perm(r[2 * jj + 1][e >> 1], r[2 * jj][e >> 1], (e & 1) ? 0x07060302u : 0x05040100u);
  return a;
}

__device__ __forceinline__ v4u trfrag(const LAS unsigned char* lo, const LAS unsigned char* hi) {
  const v2u a = __builtin_bit_cast(v2u, __builtin_amdgcn_ds_read_tr16_b64_v4i16((LAS s16x4*)lo));
  const v2u b = __builtin_bit_cast(v2u, __builtin_amdgcn_ds_read_tr16_b64_v4i16((LAS s16x4*)hi));
  return v4u{a[0], a[1], b[0], b[1]};
}
constexpr int OOB = 0x7ffffff0;

template <int WIN, int DYN, int NSTATIC, int WOLAST, int STAMPS>
__global__ void __launch_bounds__(NTHR) k_fused(Params p) {
  static_assert(WIN % 8 == 0 && WIN >= 16 && WIN <= 56, "window in 1 KiB pieces");
  extern __shared__ __attribute__((aligned(16))) unsigned char lds_raw[];
  LAS unsigned char* L = (LAS unsigned char*)lds_raw;
  volatile LAS unsigned* ctl = (volatile LAS unsigned*)(L + L_CTL);
  const u64 t_start = rt();
  const int bid = blockIdx.x, br = bid >> 7, grp = bid >> 5, kvh = grp & 3, gi = bid & 31, b = gi >> 4, split = bid & 15;
  const int tid = threadIdx.x, lane = tid & 63, wave = (int)rfl(tid >> 6);
  const int c = lane & 15, g = lane >> 4;
  const int qslice = 2 * ((gi & 7) + 8 * (gi >> 4)) + ((gi >> 3) & 1);
  static_assert(WOLAST == 1, "");
  constexpr int NS = DYN ? NSTATIC : TILES / 16;
  constexpr int NDYN = TILES - 16 * NS;
  constexpr int KV0 = WOLAST ? 4 : 8;
  constexpr unsigned END_UNKNOWN = 0x7fffffffu;
  u64* stamps = (u64*)(p.ws + WS_STAMP) + bid * 32;
  u64 sv[8] = {};
  auto stamp = [&](int i) { if constexpr (STAMPS) sv[i] = rt(); };
  auto flush = [&](int base, int n) { if constexpr (STAMPS) { if (lane == 0) for (int i = 0; i < n; ++i) stamps[base + i] = sv[i]; } };

  auto spin = [&](auto&& cond, bool nap) -> bool {
    for (unsigned it = 1;; ++it) {
      if (cond()) return true;
      if (nap) __builtin_amdgcn_s_sleep(1);
      if ((it & 255u) == 0u) {
        if (rfl(ctl[W_ABORT])) return false;
        if (rt() - t_start > TCAP) { ctl[W_ABORT] = 1u; return false; }
      }
    }
  };

  if (wave == 1) { ctl[lane] = (lane == W_END) ? (DYN ? END_UNKNOWN : (unsigned)(KV0 + NS)) : 0u; LDS_WAIT(); }

  if (wave == 0) {
    __builtin_amdgcn_s_setprio(3);
    int groups = 0, pub = 0, fill = 0;
    bool ok = true;
    auto pace = [&]() {
      vmwait<WIN - 8>();
      if (2 * pub + 2 <= groups - (WIN / 8 - 1)) { ctl[W_FULL + (pub & (NSLOT - 1))] = (unsigned)(pub + 1); ++pub; }
    };
    auto drain = [&]() { vmwait<0>(); for (; pub < fill; ++pub) ctl[W_FULL + (pub & (NSLOT - 1))] = (unsigned)(pub + 1); };
    auto room = [&]() {
      if (fill >= NSLOT) {
        const unsigned need = (unsigned)(fill - NSLOT + 1);
        if (rfl(ctl[W_FREE + (fill & (NSLOT - 1))]) < need) {
          u64 tp = 0;
          if constexpr (STAMPS) tp = rt();
          drain();
          ok = ok && spin([&] { return rfl(ctl[W_FREE + (fill & (NSLOT - 1))]) >= need; }, true);
          if constexpr (STAMPS) { sv[5] += rt() - tp; sv[6] += 100; }
        }
        __builtin_amdgcn_fence(__ATOMIC_ACQUIRE, "workgroup");
      }
    };
#define GLDS(src, dst, imm) __builtin_amdgcn_global_load_lds((const GAS unsigned*)(src), (LAS unsigned*)(dst), 16, (imm), 2)
    auto weight_fill = [&](const unsigned char* base, unsigned voff, size_t pstride, bool paced) {
      LAS unsigned char* dst = L + L_RING + (fill & (NSLOT - 1)) * SLOT;
#pragma unroll
      for (int h = 0; h < 2; ++h) {
        if (paced) pace();
#pragma unroll
        for (int j = 0; j < 8; ++j) GLDS(base + (size_t)(8 * h + j) * pstride + voff, dst + (8 * h + j) * 1024, 0);
        ++groups;
      }
      ++fill;
    };
    const unsigned char* wq = (const unsigned char*)p.wq[br] + kvh * 2048 + qslice * 64;
    const unsigned wq_off = (unsigned)(lane >> 2) * 8192u + (unsigned)(lane & 3) * 16u;
    const unsigned char* wo = (const unsigned char*)p.wo[br] + (size_t)((kvh * 8 + (gi & 7)) * 128) * 2048 + (gi >> 3) * 512;
    const unsigned wo_off = (unsigned)(lane >> 5) * 2048u + (unsigned)(lane & 31) * 16u;
    weight_fill(wq, wq_off, 16 * 8192, false);
    if (WIN >= 32) weight_fill(wq + (size_t)256 * 8192, wq_off, 16 * 8192, false);
    __builtin_amdgcn_s_barrier();
    stamp(3);
    for (int i = WIN >= 32 ? 2 : 1; i < 4; ++i) weight_fill(wq + (size_t)i * 256 * 8192, wq_off, 16 * 8192, true);
    stamp(4);

    const size_t sbase = (size_t)(b * 4 + kvh) * SK * DH * 2;
    const unsigned char* kb = (const unsigned char*)p.k[br] + sbase;
    const unsigned char* vb = (const unsigned char*)p.v[br] + sbase;
    unsigned koff[4];
#pragma unroll
    for (int j = 0; j < 4; ++j) koff[j] = (unsigned)g * 256u + (unsigned)((c ^ ((4 * j + g) & SWZ)) * 16);
    const unsigned voff = (unsigned)lane * 16u;
    auto kv_fill = [&](unsigned tile) {
      LAS unsigned char* dst = L + L_RING + (fill & (NSLOT - 1)) * SLOT;
      const unsigned char* ks = kb + (size_t)tile * 8192;
      const unsigned char* vs = vb + (size_t)tile * 8192;
      pace();
#pragma unroll
      for (int j = 0; j < 2; ++j) {
        GLDS(ks + j * 4096 + koff[0], dst + j * 4096, 0); GLDS(ks + j * 4096 + koff[1], dst + j * 4096, 1024);
        GLDS(ks + j * 4096 + koff[2], dst + j * 4096, 2048); GLDS(ks + j * 4096 + koff[3], dst + j * 4096, 3072);
      }
      ++groups;
      pace();
#pragma unroll
      for (int j = 0; j < 2; ++j) {
        GLDS(vs + j * 4096 + voff, dst + 8192 + j * 4096, 0); GLDS(vs + j * 4096 + voff, dst + 8192 + j * 4096, 1024);
        GLDS(vs + j * 4096 + voff, dst + 8192 + j * 4096, 2048); GLDS(vs + j * 4096 + voff, dst + 8192 + j * 4096, 3072);
      }
      ++groups;
      ++fill;
    };
#pragma unroll 1
    for (int i = 0; i < NS && ok; ++i) {
      room();
      if (!ok) break;
      if (DYN) ctl[W_PROG] = (unsigned)(i + 1);
      if (i == 24) stamp(7);
      kv_fill((unsigned)(split * NS + i));
    }
    if constexpr (DYN) {
#pragma unroll 1
      for (int j = 0; ok; ++j) {
        unsigned t = rfl(ctl[W_TKT + (j & 7)]);
        if ((t >> 16) != (unsigned)(j + 1)) {
          drain();
          ok = spin([&] { t = rfl(ctl[W_TKT + (j & 7)]); return (t >> 16) == (unsigned)(j + 1); }, false);
          if (!ok) break;
        }
        t &= 0xffffu;
        if (t >= (unsigned)NDYN) break;
        room();
        if (!ok) break;
        ctl[W_PROG] = (unsigned)(NS + j + 1);
        kv_fill(16u * NS + t);
      }
      ctl[W_END] = (unsigned)fill;
    }
    stamp(0);
    sv[2] = (u64)fill;
    if (WOLAST) {
#pragma unroll 1
      for (int i = 0; i < 4 && ok; ++i) {
        room();
        if (!ok) break;
        weight_fill(wo + (size_t)i * 32 * 2048, wo_off, 2 * 2048, true);
      }
    }
    drain();
    stamp(1);
    flush(8, 8);
    return;
  }

  const __amdgpu_buffer_rsrc_t rs = __builtin_amdgcn_make_buffer_rsrc(p.ws, 0, WS_END, 0x00020000);
  auto ld = [&](int off) -> v4u { return __builtin_amdgcn_raw_buffer_load_b128(rs, off, 0, 16); };
  auto st = [&](v4u v, int off) { __builtin_amdgcn_raw_buffer_store_b128(v, rs, off, 0, 16); };
  auto lds_add = [&](int w) { if (lane == 0) __hip_atomic_fetch_add((LAS unsigned*)&ctl[w], 1u, __ATOMIC_RELAXED, __HIP_MEMORY_SCOPE_WORKGROUP); };
  auto gather = [&](auto& v, auto& off, unsigned tA, unsigned tB, bool nap) -> bool {
    constexpr int N = sizeof(v) / sizeof(v[0]);
#pragma unroll
    for (int j = 0; j < N; ++j) v[j] = v2u{0u, 0u};
    return spin([&] {
      v4u t[N];
#pragma unroll
      for (int j = 0; j < N; ++j) t[j] = ld(off[j]);
      __builtin_amdgcn_sched_barrier(0);
      bool pend = false;
#pragma unroll
      for (int j = 0; j < N; ++j) {
        const bool hit = (t[j][1] == tA) & (t[j][3] == tB) & (off[j] != OOB);
        v[j][0] = hit ? t[j][0] : v[j][0];
        v[j][1] = hit ? t[j][2] : v[j][1];
        off[j] = hit ? OOB : off[j];
        pend = pend | (off[j] != OOB);
      }
      return __any(pend) == 0;
    }, nap);
  };

  if (wave == NCONS + 1) {
    const int tkoff = WS_TICKET + 256 * (bid >> 4);
    __builtin_amdgcn_s_barrier();
    if (DYN) __builtin_amdgcn_raw_buffer_store_b32(0u, rs, (split == 0 && lane == 0) ? tkoff : OOB, 0, 16);
    const u64 ep = __hip_atomic_load((GAS u64*)(p.ws + WS_EPOCH + 256 * br), __ATOMIC_RELAXED, __HIP_MEMORY_SCOPE_AGENT);
    const unsigned tA = rfl((unsigned)ep) + 1u, tB = rfl((unsigned)(ep >> 32)) + 1u;
    vmwait<0>();
    ctl[W_TAGA] = tA; ctl[W_TAGB] = tB;
    LDS_WAIT();
    lds_add(W_QCNT);
    if (!spin([&] { return rfl(ctl[W_QPUB]) != 0u; }, true)) return;
    {
      v2u qv[4];
      int off[4];
#pragma unroll
      for (int j = 0; j < 4; ++j) off[j] = WS_Q + (grp * 2 + b) * 4096 + lane * 16 + 1024 * j;
      if (!gather(qv, off, tA, tB, true)) return;
#pragma unroll
      for (int j = 0; j < 4; ++j) *(LAS v2u*)(L + L_QIMG + j * 512 + lane * 8) = qv[j];
      LDS_WAIT();
      ctl[W_QRDY] = 1u;
    }
    stamp(0);
    if constexpr (DYN) {
      constexpr int AHEAD = 4;
      const int myoff = lane == 0 ? tkoff : OOB;
      auto claim = [&]() -> unsigned { return (unsigned)__builtin_amdgcn_raw_ptr_buffer_atomic_add_i32(1, rs, myoff, 0, 0); };
      auto gate = [&](int j) { return spin([&] { return (int)rfl(ctl[W_PROG]) + AHEAD >= NS + j + 1; }, true); };
      auto post = [&](int j, unsigned t) -> bool {
        t = rfl(t);
        const unsigned tt = t < (unsigned)NDYN ? t : 0xffffu;
        ctl[W_TKT + (j & 7)] = ((unsigned)(j + 1) << 16) | tt;
        return t < (unsigned)NDYN;
      };
      if (!gate(0)) return;
      unsigned t0 = claim();
      if (!gate(1)) return;
      unsigned t1 = claim();
#pragma unroll 1
      for (int j = 0; j <= NDYN; j += 2) {
        if (!post(j, t0)) break;
        if (!gate(j + 2)) return;
        t0 = claim();
        if (!post(j + 1, t1)) break;
        if (!gate(j + 3)) return;
        t1 = claim();
      }
    }
    if (!spin([&] { return rfl(ctl[W_ORDY]) >= (unsigned)NCONS; }, true)) return;
    stamp(1);
    v2u r[4];
    int off[4];
#pragma unroll
    for (int j = 0; j < 4; ++j) off[j] = WS_H2 + bid * 4096 + lane * 16 + 1024 * j;
    if (!gather(r, off, tA, tB, false)) return;
    float s0 = (f(r[0][0]) + f(r[1][0])) + (f(r[2][0]) + f(r[3][0])), s1 = (f(r[0][1]) + f(r[1][1])) + (f(r[2][1]) + f(r[3][1]));
    s0 += dpp<ROR8>(s0); s1 += dpp<ROR8>(s1);
    s0 = xrow_sum(s0); s1 = xrow_sum(s1);
    if (lane < 8) {
      const int oc = bid & 127;
      const int col = 64 * (oc >> 3) + 16 * (lane >> 1) + 4 * ((oc >> 1) & 3) + 2 * (oc & 1);
      *(GAS unsigned*)(p.out + (lane & 1) * 2048 + br * 1024 + col) = cvtpk(s0, s1);
    }
    if ((bid & 127) == 0 && lane == 0)
      __hip_atomic_store((GAS u64*)(p.ws + WS_EPOCH + 256 * br), ((u64)tB << 32) | tA, __ATOMIC_RELAXED, __HIP_MEMORY_SCOPE_AGENT);
    stamp(2);
    flush(16, 3);
    return;
  }

  const int ci = wave - 1;
  const int tq = (lane >> 2) & 3, tp = lane & 3;
  v4u xf[8];
#pragma unroll
  for (int ks = 0; ks < 8; ++ks) xf[ks] = *(const GAS v4u*)(p.x[br] + (c & 1) * 1024 + ci * 256 + ks * 32 + 8 * g);
  __builtin_amdgcn_s_barrier();
  stamp(1);
  auto wait_full = [&](int fl) -> bool {
    const bool r = spin([&] { return rfl(ctl[W_FULL + (fl & (NSLOT - 1))]) >= (unsigned)(fl + 1); }, true);
    __builtin_amdgcn_fence(__ATOMIC_ACQUIRE, "workgroup");
    return r;
  };
  const v4u zero4 = {0u, 0u, 0u, 0u};

  {
    if (!wait_full(ci)) return;
    const LAS unsigned char* s = L + L_RING + ci * SLOT + (8 * g + tq) * 64 + tp * 8;
    v4u w0[8], w1[8];
#pragma unroll
    for (int ks = 0; ks < 8; ++ks) { w0[ks] = trfrag(s + ks * 2048, s + ks * 2048 + 256); w1[ks] = trfrag(s + ks * 2048 + 32, s + ks * 2048 + 288); }
    LDS_WAIT();
    ctl[W_FREE + ci] = (unsigned)(ci + 1);
    PIN8(w0); PIN8(w1);
    f32x4 a0 = {0.f, 0.f, 0.f, 0.f}, a1 = {0.f, 0.f, 0.f, 0.f};
#pragma unroll
    for (int ks = 0; ks < 8; ++ks) { a0 = mfma(w0[ks], xf[ks], a0); a1 = mfma(w1[ks], xf[ks], a1); }
    if (c < 2) {
      LAS unsigned char* d = L + L_QRED + ci * 256 + c * 128 + g * 16;
      *(LAS f32x4*)d = a0; *(LAS f32x4*)(d + 64) = a1;
    }
    LDS_WAIT();
    lds_add(W_QCNT);
  }
  stamp(2);
  if (!spin([&] { return rfl(ctl[W_QCNT]) >= (unsigned)(NCONS + 1); }, false)) return;
  __builtin_amdgcn_fence(__ATOMIC_ACQUIRE, "workgroup");
  const unsigned tA = rfl(ctl[W_TAGA]), tB = rfl(ctl[W_TAGB]);
  if (ci == 0) {
    f32x4 a = *(const LAS f32x4*)(L + L_QRED + c * 16);
#pragma unroll
    for (int w = 1; w < NCONS; ++w) a += *(const LAS f32x4*)(L + L_QRED + w * 256 + c * 16);
    st(v4u{cvtpk(a[0], a[1]), tA, cvtpk(a[2], a[3]), tB}, lane < 16 ? WS_Q + (grp * 2 + (c >> 3)) * 4096 + qslice * 128 + (c & 7) * 16 : OOB);
    ctl[W_QPUB] = 1u;
  }
  stamp(3);
  if (!spin([&] { return rfl(ctl[W_QRDY]) != 0u; }, true)) return;
  __builtin_amdgcn_fence(__ATOMIC_ACQUIRE, "workgroup");
  v4u qf[4];
#pragma unroll
  for (int s = 0; s < 4; ++s) {
    const v4u t = *(const LAS v4u*)(L + L_QIMG + (c & 7) * 256 + s * 64 + g * 16);
    qf[s] = c < 8 ? t : zero4;
  }
  stamp(4);

  f32x4 acc[8];
#pragma unroll
  for (int e = 0; e < 8; ++e) acc[e] = f32x4{0.f, 0.f, 0.f, 0.f};
  float m = -INFINITY, l = 0.f;
  unsigned kra[4];
#pragma unroll
  for (int s = 0; s < 4; ++s) kra[s] = (unsigned)c * 256u + (unsigned)(((4 * s + g) ^ (c & SWZ)) * 16);
  const unsigned vra = 8192u + (unsigned)g * 1024u + (unsigned)c * 16u;
  int fl = KV0 + ci;
#pragma unroll 1
  for (;; fl += NCONS) {
    bool done = false;
    if (!spin([&] {
          const unsigned fu = rfl(ctl[W_FULL + (fl & (NSLOT - 1))]);
          const unsigned en = DYN ? rfl(ctl[W_END]) : (unsigned)(KV0 + NS);
          done = (unsigned)fl >= en;
          return done || fu >= (unsigned)(fl + 1);
        }, true)) return;
    if (done) break;
    __builtin_amdgcn_fence(__ATOMIC_ACQUIRE, "workgroup");
    const LAS unsigned char* s = L + L_RING + (fl & (NSLOT - 1)) * SLOT;
    v4u K[8], V[8];
#pragma unroll
    for (int j = 0; j < 8; ++j) K[j] = *(const LAS v4u*)(s + kra[j & 3] + (j >> 2) * 4096);
#pragma unroll
    for (int j = 0; j < 8; ++j) V[j] = *(const LAS v4u*)(s + vra + (j >> 2) * 4096 + (j & 3) * 256);
    LDS_WAIT();
    ctl[W_FREE + (fl & (NSLOT - 1))] = (unsigned)(fl + 1);
    PIN8(K); PIN8(V);
    f32x4 s0 = {0.f, 0.f, 0.f, 0.f}, s1 = {0.f, 0.f, 0.f, 0.f};
#pragma unroll
    for (int j = 0; j < 4; ++j) { s0 = mfma(K[j], qf[j], s0); s1 = mfma(K[4 + j], qf[j], s1); }
    float tm = fmaxf(fmaxf(fmaxf(s0[0], s0[1]), fmaxf(s0[2], s0[3])), fmaxf(fmaxf(s1[0], s1[1]), fmaxf(s1[2], s1[3])));
    tm = xrow_max(tm) * C2;
    const float mn = fmaxf(m, tm);
    const float alpha = __builtin_amdgcn_exp2f(m - mn);
    m = mn;
    float pr[8];
#pragma unroll
    for (int i = 0; i < 4; ++i) { pr[i] = __builtin_amdgcn_exp2f(fmaf(s0[i], C2, -mn)); pr[4 + i] = __builtin_amdgcn_exp2f(fmaf(s1[i], C2, -mn)); }
    l = l * alpha + (((pr[0] + pr[1]) + (pr[2] + pr[3])) + ((pr[4] + pr[5]) + (pr[6] + pr[7])));
    const v4u pf = {cvtpk(pr[0], pr[1]), cvtpk(pr[2], pr[3]), cvtpk(pr[4], pr[5]), cvtpk(pr[6], pr[7])};
#pragma unroll
    for (int e = 0; e < 8; ++e) acc[e] = mfma(tfrag(V, e), pf, acc[e] * alpha);
  }

  stamp(5);
  l = xrow_sum(l);
  if (c < 8) {
    LAS unsigned char* row = L + L_MRG + (ci * 8 + c) * MROW + g * 128;
#pragma unroll
    for (int r = 0; r < 4; ++r) {
      *(LAS f32x4*)(row + r * 32) = f32x4{acc[0][r], acc[1][r], acc[2][r], acc[3][r]};
      *(LAS f32x4*)(row + r * 32 + 16) = f32x4{acc[4][r], acc[5][r], acc[6][r], acc[7][r]};
    }
    if (g == 0) { LAS float* mm = (LAS float*)(L + L_MML) + (ci * 8 + c) * 2; mm[0] = m; mm[1] = l; }
  }
  LDS_WAIT();
  lds_add(W_MCNT);
  if (!spin([&] { return rfl(ctl[W_MCNT]) >= (unsigned)NCONS; }, false)) return;
  __builtin_amdgcn_fence(__ATOMIC_ACQUIRE, "workgroup");
  {
    const int h = 2 * ci + (lane >> 5), l5 = lane & 31;
    float mw[NCONS], lw[NCONS];
    f32x4 aw[NCONS];
#pragma unroll
    for (int w = 0; w < NCONS; ++w) {
      const LAS float* mm = (const LAS float*)(L + L_MML) + (w * 8 + h) * 2;
      mw[w] = mm[0]; lw[w] = mm[1];
      aw[w] = *(const LAS f32x4*)(L + L_MRG + (w * 8 + h) * MROW + l5 * 16);
    }
    const float M = fmaxf(fmaxf(mw[0], mw[1]), fmaxf(mw[2], mw[3]));
    float Ls = 0.f;
    f32x4 o = {0.f, 0.f, 0.f, 0.f};
#pragma unroll
    for (int w = 0; w < NCONS; ++w) { const float wt = __builtin_amdgcn_exp2f(mw[w] - M); Ls = fmaf(wt, lw[w], Ls); o += aw[w] * wt; }
    const int rec = WS_H1 + ((grp * 32 + h * 4 + (l5 >> 3)) * 32 + gi) * 256 + (l5 & 7) * 16;
    st(v4u{u(o[0]), tA, u(o[1]), tB}, rec);
    st(v4u{u(o[2]), tA, u(o[3]), tB}, rec + 128);
    st(v4u{u(M), tA, u(Ls), tB}, l5 == 0 ? WS_ML + ((grp * 8 + h) * 32 + gi) * 16 : OOB);
  }
  stamp(6);

  {
    const int pb = c & 1, sp = c >> 1;
    v2u d[10];
    int off[10];
    const int rec = WS_H1 + ((grp * 32 + (gi & 7) * 4 + ci) * 32 + pb * 16 + 2 * sp) * 256 + g * 16;
    const int mlo = WS_ML + ((grp * 8 + (gi & 7)) * 32 + pb * 16 + 2 * sp) * 16;
#pragma unroll
    for (int j = 0; j < 10; ++j) off[j] = j < 8 ? rec + 64 * j : mlo + 16 * (j - 8);
    if (!gather(d, off, tA, tB, false)) return;
    float M = fmaxf(f(d[8][0]), f(d[9][0]));
    M = fmaxf(M, dpp<ROR2>(M)); M = fmaxf(M, dpp<ROR4>(M)); M = fmaxf(M, dpp<ROR8>(M));
    const float w0 = __builtin_amdgcn_exp2f(f(d[8][0]) - M), w1 = __builtin_amdgcn_exp2f(f(d[9][0]) - M);
    float Ls = w0 * f(d[8][1]) + w1 * f(d[9][1]);
    Ls += dpp<ROR2>(Ls); Ls += dpp<ROR4>(Ls); Ls += dpp<ROR8>(Ls);
    const float inv = 1.0f / Ls;
    v4u of;
#pragma unroll
    for (int j = 0; j < 4; ++j) {
      float o0 = w0 * f(d[j][0]) + w1 * f(d[4 + j][0]), o1 = w0 * f(d[j][1]) + w1 * f(d[4 + j][1]);
      o0 += dpp<ROR2>(o0); o1 += dpp<ROR2>(o1); o0 += dpp<ROR4>(o0); o1 += dpp<ROR4>(o1); o0 += dpp<ROR8>(o0); o1 += dpp<ROR8>(o1);
      of[j] = cvtpk(o0 * inv, o1 * inv);
    }
    *(LAS v4u*)(L + L_OFRAG + ci * 1024 + lane * 16) = of;
    LDS_WAIT();
    lds_add(W_ORDY);
  }
  stamp(1);

  const int wo0 = DYN ? (int)rfl(ctl[W_END]) : KV0 + NS;
  if (!wait_full(wo0 + 3)) return;
  v4u wf[4][4];
#pragma unroll
  for (int ks = 0; ks < 4; ++ks) {
    const LAS unsigned char* s = L + L_RING + ((wo0 + ks) & (NSLOT - 1)) * SLOT + (16 * (tq >> 1) + 4 * g + (tq & 1)) * 512 + ci * 128 + tp * 8;
#pragma unroll
    for (int e = 0; e < 4; ++e) wf[ks][e] = trfrag(s + e * 32, s + e * 32 + 1024);
  }
  if (!spin([&] { return rfl(ctl[W_ORDY]) >= (unsigned)NCONS; }, false)) return;
  __builtin_amdgcn_fence(__ATOMIC_ACQUIRE, "workgroup");
  f32x4 a0 = {0.f, 0.f, 0.f, 0.f}, a1 = {0.f, 0.f, 0.f, 0.f};
#pragma unroll
  for (int ks = 0; ks < 4; ++ks) {
    const v4u of = *(const LAS v4u*)(L + L_OFRAG + ks * 1024 + lane * 16);
#pragma unroll
    for (int e = 0; e < 4; e += 2) {
      a0 = mfma(wf[ks][e], (c >> 1) == e ? of : zero4, a0);
      a1 = mfma(wf[ks][e + 1], (c >> 1) == e + 1 ? of : zero4, a1);
    }
  }
  a0 += a1;
  {
    const int o0 = c < 8 ? WS_H2 + (br * 128 + (((gi >> 3) * 4 + ci) * 4 + g) * 2) * 4096 + (kvh * 8 + (gi & 7)) * 128 + c * 16 : OOB - 4096;
    st(v4u{u(a0[0]), tA, u(a0[1]), tB}, o0);
    st(v4u{u(a0[2]), tA, u(a0[3]), tB}, o0 + 4096);
  }
  stamp(7);
  sv[0] = t_start;
  if (ci == 0) flush(0, 8);
}

template <int WIN, int DYN, int NSTATIC, int WOLAST, int STAMPS>
static void launch(void* const* d_in, void* d_out, void* d_ws, hipStream_t stream) {
  Params p;
  for (int br = 0; br < 2; ++br) {
    p.x[br] = (const u16*)d_in[5 * br + 0]; p.k[br] = (const u16*)d_in[5 * br + 1]; p.v[br] = (const u16*)d_in[5 * br + 2];
    p.wq[br] = (const u16*)d_in[5 * br + 3]; p.wo[br] = (const u16*)d_in[5 * br + 4];
  }
  p.out = (u16*)d_out; p.ws = (unsigned char*)d_ws;
  (void)hipFuncSetAttribute((const void*)k_fused<WIN, DYN, NSTATIC, WOLAST, STAMPS>, hipFuncAttributeMaxDynamicSharedMemorySize, L_BYTES);
  hipLaunchKernelGGL((k_fused<WIN, DYN, NSTATIC, WOLAST, STAMPS>), dim3(NWG), dim3(NTHR), L_BYTES, stream, p);
}

}

extern "C" void kernel_launch(void* const* d_in, const int* in_sizes, int n_in, void* d_out, int out_size, void* d_ws, size_t ws_size,
                              hipStream_t stream) {
  fused::launch<CFG_WIN, CFG_DYN, CFG_NSTATIC, CFG_WOLAST, CFG_STAMPS>(d_in, d_out, d_ws, stream);
}
```

```cpp
#define DIAG 1
#define CFG_WIN 24
#include <hip/hip_runtime.h>
#include <hip/hip_bf16.h>
#include <cstdint>

#define GAS __attribute__((address_space(1)))
#define LAS __attribute__((address_space(3)))
typedef unsigned v4u __attribute__((ext_vector_type(4)));
typedef float f32x4 __attribute__((ext_vector_type(4)));
typedef short bf16x8 __attribute__((ext_vector_type(8)));
typedef short s16x4 __attribute__((ext_vector_type(4)));
typedef unsigned v2u __attribute__((ext_vector_type(2)));
typedef unsigned short u16;
typedef unsigned long long u64;

#define CFG_DYN 0
#define CFG_NSTATIC 48
#define CFG_WOLAST 1
#define CFG_STAMPS 0

#define SWZ 15
#define DIAGSEL true
namespace fused {

constexpr int SK = 32768, DH = 128;
constexpr int NWG = 256, NCONS = 4, NTHR = 64 * (NCONS + 2);
constexpr int NSLOT = 8, SLOT = 16384;
constexpr int TILES = SK / 32;
constexpr float C2 = 0.088388347648318440f * 1.4426950408889634f;
constexpr u64 TCAP = 2000000;

constexpr int WS_EPOCH = 0;
constexpr int WS_TICKET = 4096;
constexpr int WS_ML = 16384;
constexpr int WS_Q = 65536;
constexpr int WS_H2 = WS_Q + 8 * 2 * 4096;
constexpr int WS_H1 = WS_H2 + 256 * 4096;
constexpr int WS_END = WS_H1 + 256 * 32 * 256;
constexpr int WS_STAMP = WS_END;

constexpr int L_RING = 0;
constexpr int L_CTL = NSLOT * SLOT;
constexpr int L_QIMG = L_CTL + 256;
constexpr int L_QRED = L_QIMG + 2048;
constexpr int L_OFRAG = L_QRED + 1024;
constexpr int MROW = 528;
constexpr int L_MRG = L_OFRAG + 4096;
constexpr int L_MML = L_MRG + 4 * 8 * MROW;
constexpr int L_BYTES = L_MML + 256;
enum { W_FULL = 0, W_FREE = 16, W_ABORT = 32, W_END = 33, W_QCNT = 34, W_QRDY = 35, W_MCNT = 36, W_ORDY = 37, W_PROG = 38, W_TKT = 40, W_TAGA = 48, W_TAGB = 49, W_QPUB = 50 };
static_assert(L_BYTES <= 160 * 1024 && L_BYTES > 80 * 1024, "one workgroup per CU");

struct Params { const u16* x[2]; const u16* k[2]; const u16* v[2]; const u16* wq[2]; const u16* wo[2]; u16* out; unsigned char* ws; };

__device__ __forceinline__ u64 rt() { return __builtin_amdgcn_s_memrealtime(); }
__device__ __forceinline__ unsigned rfl(unsigned v) { return (unsigned)__builtin_amdgcn_readfirstlane((int)v); }
__device__ __forceinline__ unsigned cvtpk(float lo, float hi) {
  unsigned r; asm("v_cvt_pk_bf16_f32 %0, %1, %2" : "=v"(r) : "v"(lo), "v"(hi)); return r;
}
__device__ __forceinline__ f32x4 mfma(v4u a, v4u b, f32x4 c) {
  return __builtin_amdgcn_mfma_f32_16x16x32_bf16(__builtin_bit_cast(bf16x8, a), __builtin_bit_cast(bf16x8, b), c, 0, 0, 0);
}
__device__ __forceinline__ float f(unsigned u) { return __uint_as_float(u); }
__device__ __forceinline__ unsigned u(float x) { return __float_as_uint(x); }
__device__ __forceinline__ float xrow_max(float x) {
  auto s = __builtin_amdgcn_permlane16_swap(u(x), u(x), false, false);
  x = fmaxf(f(s[0]), f(s[1]));
  auto t = __builtin_amdgcn_permlane32_swap(u(x), u(x), false, false);
  return fmaxf(f(t[0]), f(t[1]));
}
__device__ __forceinline__ float xrow_sum(float x) {
  auto s = __builtin_amdgcn_permlane16_swap(u(x), u(x), false, false);
  x = f(s[0]) + f(s[1]);
  auto t = __builtin_amdgcn_permlane32_swap(u(x), u(x), false, false);
  return f(t[0]) + f(t[1]);
}
template <int CTRL> __device__ __forceinline__ float dpp(float x) {
  return __builtin_bit_cast(float, __builtin_amdgcn_mov_dpp(__builtin_bit_cast(int, x), CTRL, 0xf, 0xf, true));
}
constexpr int ROR2 = 0x122, ROR4 = 0x124, ROR8 = 0x128;
template <int N> __device__ __forceinline__ void vmwait() { asm volatile("s_waitcnt vmcnt(%0)" ::"i"(N) : "memory"); }
#define LDS_WAIT() asm volatile("s_waitcnt lgkmcnt(0)" ::: "memory")
#define PIN8(w) asm volatile("" : "+v"(w[0]), "+v"(w[1]), "+v"(w[2]), "+v"(w[3]), "+v"(w[4]), "+v"(w[5]), "+v"(w[6]), "+v"(w[7]) : : "memory")

__device__ __forceinline__ v4u tfrag(const v4u (&r)[8], int e) {
  v4u a;
#pragma unroll
  for (int jj = 0; jj < 4; ++jj) a[jj] = __builtin_amdgcn_perm(r[2 * jj + 1][e >> 1], r[2 * jj][e >> 1], (e & 1) ? 0x07060302u : 0x05040100u);
  return a;
}

__device__ __forceinline__ v4u trfrag(const LAS unsigned char* lo, const LAS unsigned char* hi) {
  const v2u a = __builtin_bit_cast(v2u, __builtin_amdgcn_ds_read_tr16_b64_v4i16((LAS s16x4*)lo));
  const v2u b = __builtin_bit_cast(v2u, __builtin_amdgcn_ds_read_tr16_b64_v4i16((LAS s16x4*)hi));
  return v4u{a[0], a[1], b[0], b[1]};
}
constexpr int OOB = 0x7ffffff0;

template <int WIN, int DYN, int NSTATIC, int WOLAST, int STAMPS>
__global__ void __launch_bounds__(NTHR) k_fused(Params p) {
  static_assert(WIN % 8 == 0 && WIN >= 16 && WIN <= 56, "window in 1 KiB pieces");
  extern __shared__ __attribute__((aligned(16))) unsigned char lds_raw[];
  LAS unsigned char* L = (LAS unsigned char*)lds_raw;
  volatile LAS unsigned* ctl = (volatile LAS unsigned*)(L + L_CTL);
  const u64 t_start = rt();
  const int bid = blockIdx.x, br = bid >> 7, grp = bid >> 5, kvh = grp & 3, gi = bid & 31, b = gi >> 4, split = bid & 15;
  const int tid = threadIdx.x, lane = tid & 63, wave = (int)rfl(tid >> 6);
  const int c = lane & 15, g = lane >> 4;
  const int qslice = 2 * ((gi & 7) + 8 * (gi >> 4)) + ((gi >> 3) & 1);
  static_assert(WOLAST == 1, "");
  constexpr int NS = DYN ? NSTATIC : TILES / 16;
  constexpr int NDYN = TILES - 16 * NS;
  constexpr int KV0 = WOLAST ? 4 : 8;
  constexpr unsigned END_UNKNOWN = 0x7fffffffu;
  u64* stamps = (u64*)(p.ws + WS_STAMP) + bid * 32;
  u64 sv[8] = {};
  volatile LAS unsigned* dg = (volatile LAS unsigned*)(L + L_BYTES);
  const int dbase = wave == 0 ? 8 : wave == NCONS + 1 ? 16 : wave == 1 ? 0 : 24;
  auto stamp = [&](int i) { if constexpr (STAMPS) sv[i] = rt(); if (DIAG) dg[dbase + i] = (unsigned)rt(); };
  auto flush = [&](int base, int n) { if constexpr (STAMPS) { if (lane == 0) for (int i = 0; i < n; ++i) stamps[base + i] = sv[i]; } };

  auto spin = [&](auto&& cond, bool nap) -> bool {
    for (unsigned it = 1;; ++it) {
      if (cond()) return true;
      if (nap) __builtin_amdgcn_s_sleep(1);
      if ((it & 255u) == 0u) {
        if (rfl(ctl[W_ABORT])) return false;
        if (rt() - t_start > TCAP) { ctl[W_ABORT] = 1u; return false; }
      }
    }
  };

  if (wave == 1) { ctl[lane] = (lane == W_END) ? (DYN ? END_UNKNOWN : (unsigned)(KV0 + NS)) : 0u; LDS_WAIT(); }

  if (wave == 0) {
    __builtin_amdgcn_s_setprio(3);
    int groups = 0, pub = 0, fill = 0;
    bool ok = true;
    auto pace = [&]() {
      vmwait<WIN - 8>();
      if (2 * pub + 2 <= groups - (WIN / 8 - 1)) { ctl[W_FULL + (pub & (NSLOT - 1))] = (unsigned)(pub + 1); ++pub; }
    };
    auto drain = [&]() { vmwait<0>(); for (; pub < fill; ++pub) ctl[W_FULL + (pub & (NSLOT - 1))] = (unsigned)(pub + 1); };
    auto room = [&]() {
      if (fill >= NSLOT) {
        const unsigned need = (unsigned)(fill - NSLOT + 1);
        if (rfl(ctl[W_FREE + (fill & (NSLOT - 1))]) < need) {
          u64 tp = 0;
          if constexpr (STAMPS) tp = rt();
          drain();
          ok = ok && spin([&] { return rfl(ctl[W_FREE + (fill & (NSLOT - 1))]) >= need; }, true);
          if constexpr (STAMPS) { sv[5] += rt() - tp; sv[6] += 100; }
        }
        __builtin_amdgcn_fence(__ATOMIC_ACQUIRE, "workgroup");
      }
    };
#define GLDS(src, dst, imm) __builtin_amdgcn_global_load_lds((const GAS unsigned*)(src), (LAS unsigned*)(dst), 16, (imm), 2)
    auto weight_fill = [&](const unsigned char* base, unsigned voff, size_t pstride, bool paced) {
      LAS unsigned char* dst = L + L_RING + (fill & (NSLOT - 1)) * SLOT;
#pragma unroll
      for (int h = 0; h < 2; ++h) {
        if (paced) pace();
#pragma unroll
        for (int j = 0; j < 8; ++j) GLDS(base + (size_t)(8 * h + j) * pstride + voff, dst + (8 * h + j) * 1024, 0);
        ++groups;
      }
      ++fill;
    };
    const unsigned char* wq = (const unsigned char*)p.wq[br] + kvh * 2048 + qslice * 64;
    const unsigned wq_off = (unsigned)(lane >> 2) * 8192u + (unsigned)(lane & 3) * 16u;
    const unsigned char* wo = (const unsigned char*)p.wo[br] + (size_t)((kvh * 8 + (gi & 7)) * 128) * 2048 + (gi >> 3) * 512;
    const unsigned wo_off = (unsigned)(lane >> 5) * 2048u + (unsigned)(lane & 31) * 16u;
    weight_fill(wq, wq_off, 16 * 8192, false);
    if (WIN >= 32) weight_fill(wq + (size_t)256 * 8192, wq_off, 16 * 8192, false);
    __builtin_amdgcn_s_barrier();
    stamp(3);
    for (int i = WIN >= 32 ? 2 : 1; i < 4; ++i) weight_fill(wq + (size_t)i * 256 * 8192, wq_off, 16 * 8192, true);
    stamp(4);

    const size_t sbase = (size_t)(b * 4 + kvh) * SK * DH * 2;
    const unsigned char* kb = (const unsigned char*)p.k[br] + sbase;
    const unsigned char* vb = (const unsigned char*)p.v[br] + sbase;
    unsigned koff[4];
#pragma unroll
    for (int j = 0; j < 4; ++j) koff[j] = (unsigned)g * 256u + (unsigned)((c ^ ((4 * j + g) & SWZ)) * 16);
    const unsigned voff = (unsigned)lane * 16u;
    auto kv_fill = [&](unsigned tile) {
      LAS unsigned char* dst = L + L_RING + (fill & (NSLOT - 1)) * SLOT;
      const unsigned char* ks = kb + (size_t)tile * 8192;
      const unsigned char* vs = vb + (size_t)tile * 8192;
      pace();
#pragma unroll
      for (int j = 0; j < 2; ++j) {
        GLDS(ks + j * 4096 + koff[0], dst + j * 4096, 0); GLDS(ks + j * 4096 + koff[1], dst + j * 4096, 1024);
        GLDS(ks + j * 4096 + koff[2], dst + j * 4096, 2048); GLDS(ks + j * 4096 + koff[3], dst + j * 4096, 3072);
      }
      ++groups;
      pace();
#pragma unroll
      for (int j = 0; j < 2; ++j) {
        GLDS(vs + j * 4096 + voff, dst + 8192 + j * 4096, 0); GLDS(vs + j * 4096 + voff, dst + 8192 + j * 4096, 1024);
        GLDS(vs + j * 4096 + voff, dst + 8192 + j * 4096, 2048); GLDS(vs + j * 4096 + voff, dst + 8192 + j * 4096, 3072);
      }
      ++groups;
      ++fill;
    };
#pragma unroll 1
    for (int i = 0; i < NS && ok; ++i) {
      room();
      if (!ok) break;
      if (DYN) ctl[W_PROG] = (unsigned)(i + 1);
      if (i == 24) stamp(7);
      kv_fill((unsigned)(split * NS + i));
    }
    if constexpr (DYN) {
#pragma unroll 1
      for (int j = 0; ok; ++j) {
        unsigned t = rfl(ctl[W_TKT + (j & 7)]);
        if ((t >> 16) != (unsigned)(j + 1)) {
          drain();
          ok = spin([&] { t = rfl(ctl[W_TKT + (j & 7)]); return (t >> 16) == (unsigned)(j + 1); }, false);
          if (!ok) break;
        }
        t &= 0xffffu;
        if (t >= (unsigned)NDYN) break;
        room();
        if (!ok) break;
        ctl[W_PROG] = (unsigned)(NS + j + 1);
        kv_fill(16u * NS + t);
      }
      ctl[W_END] = (unsigned)fill;
    }
    stamp(0);
    sv[2] = (u64)fill;
    if (WOLAST) {
#pragma unroll 1
      for (int i = 0; i < 4 && ok; ++i) {
        room();
        if (!ok) break;
        weight_fill(wo + (size_t)i * 32 * 2048, wo_off, 2 * 2048, true);
      }
    }
    drain();
    stamp(1);
    flush(8, 8);
    return;
  }

  const __amdgpu_buffer_rsrc_t rs = __builtin_amdgcn_make_buffer_rsrc(p.ws, 0, WS_END, 0x00020000);
  auto ld = [&](int off) -> v4u { return __builtin_amdgcn_raw_buffer_load_b128(rs, off, 0, 16); };
  auto st = [&](v4u v, int off) { __builtin_amdgcn_raw_buffer_store_b128(v, rs, off, 0, 16); };
  auto lds_add = [&](int w) { if (lane == 0) __hip_atomic_fetch_add((LAS unsigned*)&ctl[w], 1u, __ATOMIC_RELAXED, __HIP_MEMORY_SCOPE_WORKGROUP); };
  auto gather = [&](auto& v, auto& off, unsigned tA, unsigned tB, bool nap) -> bool {
    constexpr int N = sizeof(v) / sizeof(v[0]);
#pragma unroll
    for (int j = 0; j < N; ++j) v[j] = v2u{0u, 0u};
    return spin([&] {
      v4u t[N];
#pragma unroll
      for (int j = 0; j < N; ++j) t[j] = ld(off[j]);
      __builtin_amdgcn_sched_barrier(0);
      bool pend = false;
#pragma unroll
      for (int j = 0; j < N; ++j) {
        const bool hit = (t[j][1] == tA) & (t[j][3] == tB) & (off[j] != OOB);
        v[j][0] = hit ? t[j][0] : v[j][0];
        v[j][1] = hit ? t[j][2] : v[j][1];
        off[j] = hit ? OOB : off[j];
        pend = pend | (off[j] != OOB);
      }
      return __any(pend) == 0;
    }, nap);
  };

  if (wave == NCONS + 1) {
    const int tkoff = WS_TICKET + 256 * (bid >> 4);
    __builtin_amdgcn_s_barrier();
    if (DYN) __builtin_amdgcn_raw_buffer_store_b32(0u, rs, (split == 0 && lane == 0) ? tkoff : OOB, 0, 16);
    const u64 ep = __hip_atomic_load((GAS u64*)(p.ws + WS_EPOCH + 256 * br), __ATOMIC_RELAXED, __HIP_MEMORY_SCOPE_AGENT);
    const unsigned tA = rfl((unsigned)ep) + 1u, tB = rfl((unsigned)(ep >> 32)) + 1u;
    vmwait<0>();
    ctl[W_TAGA] = tA; ctl[W_TAGB] = tB;
    LDS_WAIT();
    lds_add(W_QCNT);
    if (!spin([&] { return rfl(ctl[W_QPUB]) != 0u; }, true)) return;
    {
      v2u qv[4];
      int off[4];
#pragma unroll
      for (int j = 0; j < 4; ++j) off[j] = WS_Q + (grp * 2 + b) * 4096 + lane * 16 + 1024 * j;
      if (!gather(qv, off, tA, tB, true)) return;
#pragma unroll
      for (int j = 0; j < 4; ++j) *(LAS v2u*)(L + L_QIMG + j * 512 + lane * 8) = qv[j];
      LDS_WAIT();
      ctl[W_QRDY] = 1u;
    }
    stamp(0);
    if constexpr (DYN) {
      constexpr int AHEAD = 4;
      const int myoff = lane == 0 ? tkoff : OOB;
      auto claim = [&]() -> unsigned { return (unsigned)__builtin_amdgcn_raw_ptr_buffer_atomic_add_i32(1, rs, myoff, 0, 0); };
      auto gate = [&](int j) { return spin([&] { return (int)rfl(ctl[W_PROG]) + AHEAD >= NS + j + 1; }, true); };
      auto post = [&](int j, unsigned t) -> bool {
        t = rfl(t);
        const unsigned tt = t < (unsigned)NDYN ? t : 0xffffu;
        ctl[W_TKT + (j & 7)] = ((unsigned)(j + 1) << 16) | tt;
        return t < (unsigned)NDYN;
      };
      if (!gate(0)) return;
      unsigned t0 = claim();
      if (!gate(1)) return;
      unsigned t1 = claim();
#pragma unroll 1
      for (int j = 0; j <= NDYN; j += 2) {
        if (!post(j, t0)) break;
        if (!gate(j + 2)) return;
        t0 = claim();
        if (!post(j + 1, t1)) break;
        if (!gate(j + 3)) return;
        t1 = claim();
      }
    }
    if (!spin([&] { return rfl(ctl[W_ORDY]) >= (unsigned)NCONS; }, true)) return;
    stamp(1);
    v2u r[4];
    int off[4];
#pragma unroll
    for (int j = 0; j < 4; ++j) off[j] = WS_H2 + bid * 4096 + lane * 16 + 1024 * j;
    if (!gather(r, off, tA, tB, false)) return;
    float s0 = (f(r[0][0]) + f(r[1][0])) + (f(r[2][0]) + f(r[3][0])), s1 = (f(r[0][1]) + f(r[1][1])) + (f(r[2][1]) + f(r[3][1]));
    s0 += dpp<ROR8>(s0); s1 += dpp<ROR8>(s1);
    s0 = xrow_sum(s0); s1 = xrow_sum(s1);
    if (lane < 8) {
      const int oc = bid & 127;
      const int col = 64 * (oc >> 3) + 16 * (lane >> 1) + 4 * ((oc >> 1) & 3) + 2 * (oc & 1);
      *(GAS unsigned*)(p.out + (lane & 1) * 2048 + br * 1024 + col) = cvtpk(s0, s1);
    }
    if ((bid & 127) == 0 && lane == 0)
      __hip_atomic_store((GAS u64*)(p.ws + WS_EPOCH + 256 * br), ((u64)tB << 32) | tA, __ATOMIC_RELAXED, __HIP_MEMORY_SCOPE_AGENT);
    stamp(2);
    if (DIAG) {
      LDS_WAIT();
      unsigned stat = DIAG == 1 ? 0u : (rfl(dg[DIAG - 2]) - (unsigned)t_start);
      if (stat > 20000u || !(DIAGSEL)) stat = 0u;
      while (rt() - t_start < 10000ull + 4ull * stat) __builtin_amdgcn_s_sleep(2);
    }
    flush(16, 3);
    return;
  }

  const int ci = wave - 1;
  const int tq = (lane >> 2) & 3, tp = lane & 3;
  v4u xf[8];
#pragma unroll
  for (int ks = 0; ks < 8; ++ks) xf[ks] = *(const GAS v4u*)(p.x[br] + (c & 1) * 1024 + ci * 256 + ks * 32 + 8 * g);
  __builtin_amdgcn_s_barrier();
  stamp(1);
  auto wait_full = [&](int fl) -> bool {
    const bool r = spin([&] { return rfl(ctl[W_FULL + (fl & (NSLOT - 1))]) >= (unsigned)(fl + 1); }, true);
    __builtin_amdgcn_fence(__ATOMIC_ACQUIRE, "workgroup");
    return r;
  };
  const v4u zero4 = {0u, 0u, 0u, 0u};

  {
    if (!wait_full(ci)) return;
    const LAS unsigned char* s = L + L_RING + ci * SLOT + (8 * g + tq) * 64 + tp * 8;
    v4u w0[8], w1[8];
#pragma unroll
    for (int ks = 0; ks < 8; ++ks) { w0[ks] = trfrag(s + ks * 2048, s + ks * 2048 + 256); w1[ks] = trfrag(s + ks * 2048 + 32, s + ks * 2048 + 288); }
    LDS_WAIT();
    ctl[W_FREE + ci] = (unsigned)(ci + 1);
    PIN8(w0); PIN8(w1);
    f32x4 a0 = {0.f, 0.f, 0.f, 0.f}, a1 = {0.f, 0.f, 0.f, 0.f};
#pragma unroll
    for (int ks = 0; ks < 8; ++ks) { a0 = mfma(w0[ks], xf[ks], a0); a1 = mfma(w1[ks], xf[ks], a1); }
    if (c < 2) {
      LAS unsigned char* d = L + L_QRED + ci * 256 + c * 128 + g * 16;
      *(LAS f32x4*)d = a0; *(LAS f32x4*)(d + 64) = a1;
    }
    LDS_WAIT();
    lds_add(W_QCNT);
  }
  stamp(2);
  if (!spin([&] { return rfl(ctl[W_QCNT]) >= (unsigned)(NCONS + 1); }, false)) return;
  __builtin_amdgcn_fence(__ATOMIC_ACQUIRE, "workgroup");
  const unsigned tA = rfl(ctl[W_TAGA]), tB = rfl(ctl[W_TAGB]);
  if (ci == 0) {
    f32x4 a = *(const LAS f32x4*)(L + L_QRED + c * 16);
#pragma unroll
    for (int w = 1; w < NCONS; ++w) a += *(const LAS f32x4*)(L + L_QRED + w * 256 + c * 16);
    st(v4u{cvtpk(a[0], a[1]), tA, cvtpk(a[2], a[3]), tB}, lane < 16 ? WS_Q + (grp * 2 + (c >> 3)) * 4096 + qslice * 128 + (c & 7) * 16 : OOB);
    ctl[W_QPUB] = 1u;
  }
  stamp(3);
  if (!spin([&] { return rfl(ctl[W_QRDY]) != 0u; }, true)) return;
  __builtin_amdgcn_fence(__ATOMIC_ACQUIRE, "workgroup");
  v4u qf[4];
#pragma unroll
  for (int s = 0; s < 4; ++s) {
    const v4u t = *(const LAS v4u*)(L + L_QIMG + (c & 7) * 256 + s * 64 + g * 16);
    qf[s] = c < 8 ? t : zero4;
  }
  stamp(4);

  f32x4 acc[8];
#pragma unroll
  for (int e = 0; e < 8; ++e) acc[e] = f32x4{0.f, 0.f, 0.f, 0.f};
  float m = -INFINITY, l = 0.f;
  unsigned kra[4];
#pragma unroll
  for (int s = 0; s < 4; ++s) kra[s] = (unsigned)c * 256u + (unsigned)(((4 * s + g) ^ (c & SWZ)) * 16);
  const unsigned vra = 8192u + (unsigned)g * 1024u + (unsigned)c * 16u;
  int fl = KV0 + ci;
#pragma unroll 1
  for (;; fl += NCONS) {
    bool done = false;
    if (!spin([&] {
          const unsigned fu = rfl(ctl[W_FULL + (fl & (NSLOT - 1))]);
          const unsigned en = DYN ? rfl(ctl[W_END]) : (unsigned)(KV0 + NS);
          done = (unsigned)fl >= en;
          return done || fu >= (unsigned)(fl + 1);
        }, true)) return;
    if (done) break;
    __builtin_amdgcn_fence(__ATOMIC_ACQUIRE, "workgroup");
    const LAS unsigned char* s = L + L_RING + (fl & (NSLOT - 1)) * SLOT;
    v4u K[8], V[8];
#pragma unroll
    for (int j = 0; j < 8; ++j) K[j] = *(const LAS v4u*)(s + kra[j & 3] + (j >> 2) * 4096);
#pragma unroll
    for (int j = 0; j < 8; ++j) V[j] = *(const LAS v4u*)(s + vra + (j >> 2) * 4096 + (j & 3) * 256);
    LDS_WAIT();
    ctl[W_FREE + (fl & (NSLOT - 1))] = (unsigned)(fl + 1);
    PIN8(K); PIN8(V);
    f32x4 s0 = {0.f, 0.f, 0.f, 0.f}, s1 = {0.f, 0.f, 0.f, 0.f};
#pragma unroll
    for (int j = 0; j < 4; ++j) { s0 = mfma(K[j], qf[j], s0); s1 = mfma(K[4 + j], qf[j], s1); }
    float tm = fmaxf(fmaxf(fmaxf(s0[0], s0[1]), fmaxf(s0[2], s0[3])), fmaxf(fmaxf(s1[0], s1[1]), fmaxf(s1[2], s1[3])));
    tm = xrow_max(tm) * C2;
    const float mn = fmaxf(m, tm);
    const float alpha = __builtin_amdgcn_exp2f(m - mn);
    m = mn;
    float pr[8];
#pragma unroll
    for (int i = 0; i < 4; ++i) { pr[i] = __builtin_amdgcn_exp2f(fmaf(s0[i], C2, -mn)); pr[4 + i] = __builtin_amdgcn_exp2f(fmaf(s1[i], C2, -mn)); }
    l = l * alpha + (((pr[0] + pr[1]) + (pr[2] + pr[3])) + ((pr[4] + pr[5]) + (pr[6] + pr[7])));
    const v4u pf = {cvtpk(pr[0], pr[1]), cvtpk(pr[2], pr[3]), cvtpk(pr[4], pr[5]), cvtpk(pr[6], pr[7])};
#pragma unroll
    for (int e = 0; e < 8; ++e) acc[e] = mfma(tfrag(V, e), pf, acc[e] * alpha);
  }

  stamp(5);
  l = xrow_sum(l);
  if (c < 8) {
    LAS unsigned char* row = L + L_MRG + (ci * 8 + c) * MROW + g * 128;
#pragma unroll
    for (int r = 0; r < 4; ++r) {
      *(LAS f32x4*)(row + r * 32) = f32x4{acc[0][r], acc[1][r], acc[2][r], acc[3][r]};
      *(LAS f32x4*)(row + r * 32 + 16) = f32x4{acc[4][r], acc[5][r], acc[6][r], acc[7][r]};
    }
    if (g == 0) { LAS float* mm = (LAS float*)(L + L_MML) + (ci * 8 + c) * 2; mm[0] = m; mm[1] = l; }
  }
  LDS_WAIT();
  lds_add(W_MCNT);
  if (!spin([&] { return rfl(ctl[W_MCNT]) >= (unsigned)NCONS; }, false)) return;
  __builtin_amdgcn_fence(__ATOMIC_ACQUIRE, "workgroup");
  {
    const int h = 2 * ci + (lane >> 5), l5 = lane & 31;
    float mw[NCONS], lw[NCONS];
    f32x4 aw[NCONS];
#pragma unroll
    for (int w = 0; w < NCONS; ++w) {
      const LAS float* mm = (const LAS float*)(L + L_MML) + (w * 8 + h) * 2;
      mw[w] = mm[0]; lw[w] = mm[1];
      aw[w] = *(const LAS f32x4*)(L + L_MRG + (w * 8 + h) * MROW + l5 * 16);
    }
    const float M = fmaxf(fmaxf(mw[0], mw[1]), fmaxf(mw[2], mw[3]));
    float Ls = 0.f;
    f32x4 o = {0.f, 0.f, 0.f, 0.f};
#pragma unroll
    for (int w = 0; w < NCONS; ++w) { const float wt = __builtin_amdgcn_exp2f(mw[w] - M); Ls = fmaf(wt, lw[w], Ls); o += aw[w] * wt; }
    const int rec = WS_H1 + ((grp * 32 + h * 4 + (l5 >> 3)) * 32 + gi) * 256 + (l5 & 7) * 16;
    st(v4u{u(o[0]), tA, u(o[1]), tB}, rec);
    st(v4u{u(o[2]), tA, u(o[3]), tB}, rec + 128);
    st(v4u{u(M), tA, u(Ls), tB}, l5 == 0 ? WS_ML + ((grp * 8 + h) * 32 + gi) * 16 : OOB);
  }
  stamp(6);

  {
    const int pb = c & 1, sp = c >> 1;
    v2u d[10];
    int off[10];
    const int rec = WS_H1 + ((grp * 32 + (gi & 7) * 4 + ci) * 32 + pb * 16 + 2 * sp) * 256 + g * 16;
    const int mlo = WS_ML + ((grp * 8 + (gi & 7)) * 32 + pb * 16 + 2 * sp) * 16;
#pragma unroll
    for (int j = 0; j < 10; ++j) off[j] = j < 8 ? rec + 64 * j : mlo + 16 * (j - 8);
    if (!gather(d, off, tA, tB, false)) return;
    float M = fmaxf(f(d[8][0]), f(d[9][0]));
    M = fmaxf(M, dpp<ROR2>(M)); M = fmaxf(M, dpp<ROR4>(M)); M = fmaxf(M, dpp<ROR8>(M));
    const float w0 = __builtin_amdgcn_exp2f(f(d[8][0]) - M), w1 = __builtin_amdgcn_exp2f(f(d[9][0]) - M);
    float Ls = w0 * f(d[8][1]) + w1 * f(d[9][1]);
    Ls += dpp<ROR2>(Ls); Ls += dpp<ROR4>(Ls); Ls += dpp<ROR8>(Ls);
    const float inv = 1.0f / Ls;
    v4u of;
#pragma unroll
    for (int j = 0; j < 4; ++j) {
      float o0 = w0 * f(d[j][0]) + w1 * f(d[4 + j][0]), o1 = w0 * f(d[j][1]) + w1 * f(d[4 + j][1]);
      o0 += dpp<ROR2>(o0); o1 += dpp<ROR2>(o1); o0 += dpp<ROR4>(o0); o1 += dpp<ROR4>(o1); o0 += dpp<ROR8>(o0); o1 += dpp<ROR8>(o1);
      of[j] = cvtpk(o0 * inv, o1 * inv);
    }
    *(LAS v4u*)(L + L_OFRAG + ci * 1024 + lane * 16) = of;
    LDS_WAIT();
    lds_add(W_ORDY);
  }
  stamp(1);

  const int wo0 = DYN ? (int)rfl(ctl[W_END]) : KV0 + NS;
  if (!wait_full(wo0 + 3)) return;
  v4u wf[4][4];
#pragma unroll
  for (int ks = 0; ks < 4; ++ks) {
    const LAS unsigned char* s = L + L_RING + ((wo0 + ks) & (NSLOT - 1)) * SLOT + (16 * (tq >> 1) + 4 * g + (tq & 1)) * 512 + ci * 128 + tp * 8;
#pragma unroll
    for (int e = 0; e < 4; ++e) wf[ks][e] = trfrag(s + e * 32, s + e * 32 + 1024);
  }
  if (!spin([&] { return rfl(ctl[W_ORDY]) >= (unsigned)NCONS; }, false)) return;
  __builtin_amdgcn_fence(__ATOMIC_ACQUIRE, "workgroup");
  f32x4 a0 = {0.f, 0.f, 0.f, 0.f}, a1 = {0.f, 0.f, 0.f, 0.f};
#pragma unroll
  for (int ks = 0; ks < 4; ++ks) {
    const v4u of = *(const LAS v4u*)(L + L_OFRAG + ks * 1024 + lane * 16);
#pragma unroll
    for (int e = 0; e < 4; e += 2) {
      a0 = mfma(wf[ks][e], (c >> 1) == e ? of : zero4, a0);
      a1 = mfma(wf[ks][e + 1], (c >> 1) == e + 1 ? of : zero4, a1);
    }
  }
  a0 += a1;
  {
    const int o0 = c < 8 ? WS_H2 + (br * 128 + (((gi >> 3) * 4 + ci) * 4 + g) * 2) * 4096 + (kvh * 8 + (gi & 7)) * 128 + c * 16 : OOB - 4096;
    st(v4u{u(a0[0]), tA, u(a0[1]), tB}, o0);
    st(v4u{u(a0[2]), tA, u(a0[3]), tB}, o0 + 4096);
  }
  stamp(7);
  sv[0] = t_start;
  if (ci == 0) flush(0, 8);
}

template <int WIN, int DYN, int NSTATIC, int WOLAST, int STAMPS>
static void launch(void* const* d_in, void* d_out, void* d_ws, hipStream_t stream) {
  Params p;
  for (int br = 0; br < 2; ++br) {
    p.x[br] = (const u16*)d_in[5 * br + 0]; p.k[br] = (const u16*)d_in[5 * br + 1]; p.v[br] = (const u16*)d_in[5 * br + 2];
    p.wq[br] = (const u16*)d_in[5 * br + 3]; p.wo[br] = (const u16*)d_in[5 * br + 4];
  }
  p.out = (u16*)d_out; p.ws = (unsigned char*)d_ws;
  (void)hipFuncSetAttribute((const void*)k_fused<WIN, DYN, NSTATIC, WOLAST, STAMPS>, hipFuncAttributeMaxDynamicSharedMemorySize, L_BYTES + 256);
  hipLaunchKernelGGL((k_fused<WIN, DYN, NSTATIC, WOLAST, STAMPS>), dim3(NWG), dim3(NTHR), L_BYTES + 256, stream, p);
}

}

extern "C" void kernel_launch(void* const* d_in, const int* in_sizes, int n_in, void* d_out, int out_size, void* d_ws, size_t ws_size,
                              hipStream_t stream) {
  fused::launch<CFG_WIN, CFG_DYN, CFG_NSTATIC, CFG_WOLAST, CFG_STAMPS>(d_in, d_out, d_ws, stream);
}
```

```cpp
#define EXP 0
#define AUXB 2
#define CFG_WIN 24
#include <hip/hip_runtime.h>
#include <hip/hip_bf16.h>
#include <cstdint>

#define GAS __attribute__((address_space(1)))
#define LAS __attribute__((address_space(3)))
typedef unsigned v4u __attribute__((ext_vector_type(4)));
typedef float f32x4 __attribute__((ext_vector_type(4)));
typedef short bf16x8 __attribute__((ext_vector_type(8)));
typedef short s16x4 __attribute__((ext_vector_type(4)));
typedef unsigned v2u __attribute__((ext_vector_type(2)));
typedef unsigned short u16;
typedef unsigned long long u64;

#define CFG_DYN 0
#define CFG_NSTATIC 48
#define CFG_WOLAST 1
#define CFG_STAMPS 0

#define SWZ 15
namespace fused {

constexpr int SK = 32768, DH = 128;
constexpr int NWG = 256, NCONS = 4, NTHR = 64 * (NCONS + 2);
constexpr int NSLOT = 8, SLOT = 16384;
constexpr int TILES = SK / 32;
constexpr float C2 = 0.088388347648318440f * 1.4426950408889634f;
constexpr u64 TCAP = 2000000;

constexpr int WS_EPOCH = 0;
constexpr int WS_TICKET = 4096;
constexpr int WS_ML = 16384;
constexpr int WS_Q = 65536;
constexpr int WS_H2 = WS_Q + 8 * 2 * 4096;
constexpr int WS_H1 = WS_H2 + 256 * 4096;
constexpr int WS_END = WS_H1 + 256 * 32 * 256;
constexpr int WS_STAMP = WS_END;

constexpr int L_RING = 0;
constexpr int L_CTL = NSLOT * SLOT;
constexpr int L_QIMG = L_CTL + 256;
constexpr int L_QRED = L_QIMG + 2048;
constexpr int L_OFRAG = L_QRED + 1024;
constexpr int MROW = 528;
constexpr int L_MRG = L_OFRAG + 4096;
constexpr int L_MML = L_MRG + 4 * 8 * MROW;
constexpr int L_BYTES = L_MML + 256;
enum { W_FULL = 0, W_FREE = 16, W_ABORT = 32, W_END = 33, W_QCNT = 34, W_QRDY = 35, W_MCNT = 36, W_ORDY = 37, W_PROG = 38, W_TKT = 40, W_TAGA = 48, W_TAGB = 49, W_QPUB = 50 };
static_assert(L_BYTES <= 160 * 1024 && L_BYTES > 80 * 1024, "one workgroup per CU");

struct Params { const u16* x[2]; const u16* k[2]; const u16* v[2]; const u16* wq[2]; const u16* wo[2]; u16* out; unsigned char* ws; };

__device__ __forceinline__ u64 rt() { return __builtin_amdgcn_s_memrealtime(); }
__device__ __forceinline__ unsigned rfl(unsigned v) { return (unsigned)__builtin_amdgcn_readfirstlane((int)v); }
__device__ __forceinline__ unsigned cvtpk(float lo, float hi) {
  unsigned r; asm("v_cvt_pk_bf16_f32 %0, %1, %2" : "=v"(r) : "v"(lo), "v"(hi)); return r;
}
__device__ __forceinline__ f32x4 mfma(v4u a, v4u b, f32x4 c) {
  return __builtin_amdgcn_mfma_f32_16x16x32_bf16(__builtin_bit_cast(bf16x8, a), __builtin_bit_cast(bf16x8, b), c, 0, 0, 0);
}
__device__ __forceinline__ float f(unsigned u) { return __uint_as_float(u); }
__device__ __forceinline__ unsigned u(float x) { return __float_as_uint(x); }
__device__ __forceinline__ float xrow_max(float x) {
  auto s = __builtin_amdgcn_permlane16_swap(u(x), u(x), false, false);
  x = fmaxf(f(s[0]), f(s[1]));
  auto t = __builtin_amdgcn_permlane32_swap(u(x), u(x), false, false);
  return fmaxf(f(t[0]), f(t[1]));
}
__device__ __forceinline__ float xrow_sum(float x) {
  auto s = __builtin_amdgcn_permlane16_swap(u(x), u(x), false, false);
  x = f(s[0]) + f(s[1]);
  auto t = __builtin_amdgcn_permlane32_swap(u(x), u(x), false, false);
  return f(t[0]) + f(t[1]);
}
template <int CTRL> __device__ __forceinline__ float dpp(float x) {
  return __builtin_bit_cast(float, __builtin_amdgcn_mov_dpp(__builtin_bit_cast(int, x), CTRL, 0xf, 0xf, true));
}
constexpr int ROR2 = 0x122, ROR4 = 0x124, ROR8 = 0x128;
template <int N> __device__ __forceinline__ void vmwait() { asm volatile("s_waitcnt vmcnt(%0)" ::"i"(N) : "memory"); }
#define LDS_WAIT() asm volatile("s_waitcnt lgkmcnt(0)" ::: "memory")
#define PIN8(w) asm volatile("" : "+v"(w[0]), "+v"(w[1]), "+v"(w[2]), "+v"(w[3]), "+v"(w[4]), "+v"(w[5]), "+v"(w[6]), "+v"(w[7]) : : "memory")

__device__ __forceinline__ v4u tfrag(const v4u (&r)[8], int e) {
  v4u a;
#pragma unroll
  for (int jj = 0; jj < 4; ++jj) a[jj] = __builtin_amdgcn_perm(r[2 * jj + 1][e >> 1], r[2 * jj][e >> 1], (e & 1) ? 0x07060302u : 0x05040100u);
  return a;
}

__device__ __forceinline__ v4u trfrag(const LAS unsigned char* lo, const LAS unsigned char* hi) {
  const v2u a = __builtin_bit_cast(v2u, __builtin_amdgcn_ds_read_tr16_b64_v4i16((LAS s16x4*)lo));
  const v2u b = __builtin_bit_cast(v2u, __builtin_amdgcn_ds_read_tr16_b64_v4i16((LAS s16x4*)hi));
  return v4u{a[0], a[1], b[0], b[1]};
}
constexpr int OOB = 0x7ffffff0;

template <int WIN, int DYN, int NSTATIC, int WOLAST, int STAMPS>
__global__ void __launch_bounds__(NTHR) k_fused(Params p) {
  static_assert(WIN % 8 == 0 && WIN >= 16 && WIN <= 56, "window in 1 KiB pieces");
  extern __shared__ __attribute__((aligned(16))) unsigned char lds_raw[];
  LAS unsigned char* L = (LAS unsigned char*)lds_raw;
  volatile LAS unsigned* ctl = (volatile LAS unsigned*)(L + L_CTL);
  const u64 t_start = rt();
  const int bid = blockIdx.x, br = bid >> 7, grp = bid >> 5, kvh = grp & 3, gi = bid & 31, b = gi >> 4, split = bid & 15;
  const int tid = threadIdx.x, lane = tid & 63, wave = (int)rfl(tid >> 6);
  const int c = lane & 15, g = lane >> 4;
  const int qslice = 2 * ((gi & 7) + 8 * (gi >> 4)) + ((gi >> 3) & 1);
  static_assert(WOLAST == 1, "");
  constexpr int NS = DYN ? NSTATIC : TILES / 16;
  constexpr int NDYN = TILES - 16 * NS;
  constexpr int KV0 = WOLAST ? 4 : 8;
  constexpr unsigned END_UNKNOWN = 0x7fffffffu;
  u64* stamps = (u64*)(p.ws + WS_STAMP) + bid * 32;
  u64 sv[8] = {};
  auto stamp = [&](int i) { if constexpr (STAMPS) sv[i] = rt(); };
  auto flush = [&](int base, int n) { if constexpr (STAMPS) { if (lane == 0) for (int i = 0; i < n; ++i) stamps[base + i] = sv[i]; } };

  auto spin = [&](auto&& cond, bool nap) -> bool {
    for (unsigned it = 1;; ++it) {
      if (cond()) return true;
      if (nap) __builtin_amdgcn_s_sleep(1);
      if ((it & 255u) == 0u) {
        if (rfl(ctl[W_ABORT])) return false;
        if (rt() - t_start > TCAP) { ctl[W_ABORT] = 1u; return false; }
      }
    }
  };

  if (wave == 1) { ctl[lane] = (lane == W_END) ? (DYN ? END_UNKNOWN : (unsigned)(KV0 + NS)) : 0u; LDS_WAIT(); }

  if (wave == 0) {
    __builtin_amdgcn_s_setprio(3);
    int groups = 0, pub = 0, fill = 0;
    bool ok = true;
    auto pace = [&]() {
      vmwait<WIN - 8>();
      if (2 * pub + 2 <= groups - (WIN / 8 - 1)) { ctl[W_FULL + (pub & (NSLOT - 1))] = (unsigned)(pub + 1); ++pub; }
    };
    auto drain = [&]() { vmwait<0>(); for (; pub < fill; ++pub) ctl[W_FULL + (pub & (NSLOT - 1))] = (unsigned)(pub + 1); };
    auto room = [&]() {
      if (fill >= NSLOT) {
        const unsigned need = (unsigned)(fill - NSLOT + 1);
        if (rfl(ctl[W_FREE + (fill & (NSLOT - 1))]) < need) {
          u64 tp = 0;
          if constexpr (STAMPS) tp = rt();
          drain();
          ok = ok && spin([&] { return rfl(ctl[W_FREE + (fill & (NSLOT - 1))]) >= need; }, true);
          if constexpr (STAMPS) { sv[5] += rt() - tp; sv[6] += 100; }
        }
        __builtin_amdgcn_fence(__ATOMIC_ACQUIRE, "workgroup");
      }
    };
#define GLDS(src, dst, imm) __builtin_amdgcn_global_load_lds((const GAS unsigned*)(src), (LAS unsigned*)(dst), 16, (imm), AUXB)
    auto weight_fill = [&](const unsigned char* base, unsigned voff, size_t pstride, bool paced) {
      LAS unsigned char* dst = L + L_RING + (fill & (NSLOT - 1)) * SLOT;
#pragma unroll
      for (int h = 0; h < 2; ++h) {
        if (paced) pace();
#pragma unroll
        for (int j = 0; j < 8; ++j) GLDS(base + (size_t)(8 * h + j) * pstride + voff, dst + (8 * h + j) * 1024, 0);
        ++groups;
      }
      ++fill;
    };
    const unsigned char* wq = (const unsigned char*)p.wq[br] + kvh * 2048 + qslice * 64;
    const unsigned wq_off = (unsigned)(lane >> 2) * 8192u + (unsigned)(lane & 3) * 16u;
    const unsigned char* wo = (const unsigned char*)p.wo[br] + (size_t)((kvh * 8 + (gi & 7)) * 128) * 2048 + (gi >> 3) * 512;
    const unsigned wo_off = (unsigned)(lane >> 5) * 2048u + (unsigned)(lane & 31) * 16u;
    weight_fill(wq, wq_off, 16 * 8192, false);
    if (WIN >= 32) weight_fill(wq + (size_t)256 * 8192, wq_off, 16 * 8192, false);
    __builtin_amdgcn_s_barrier();
    stamp(3);
    for (int i = WIN >= 32 ? 2 : 1; i < 4; ++i) weight_fill(wq + (size_t)i * 256 * 8192, wq_off, 16 * 8192, true);
    stamp(4);

    const size_t sbase = (size_t)(b * 4 + kvh) * SK * DH * 2;
    const unsigned char* kb = (const unsigned char*)p.k[br] + sbase;
    const unsigned char* vb = (const unsigned char*)p.v[br] + sbase;
    unsigned koff[4];
#pragma unroll
    for (int j = 0; j < 4; ++j) koff[j] = (unsigned)g * 256u + (unsigned)((c ^ ((4 * j + g) & SWZ)) * 16);
    const unsigned voff = (unsigned)lane * 16u;
    auto kv_fill = [&](unsigned tile) {
      LAS unsigned char* dst = L + L_RING + (fill & (NSLOT - 1)) * SLOT;
      const unsigned char* ks = kb + (size_t)tile * 8192;
      const unsigned char* vs = vb + (size_t)tile * 8192;
      pace();
#pragma unroll
      for (int j = 0; j < 2; ++j) {
        GLDS(ks + j * 4096 + koff[0], dst + j * 4096, 0); GLDS(ks + j * 4096 + koff[1], dst + j * 4096, 1024);
        GLDS(ks + j * 4096 + koff[2], dst + j * 4096, 2048); GLDS(ks + j * 4096 + koff[3], dst + j * 4096, 3072);
      }
      ++groups;
      pace();
#pragma unroll
      for (int j = 0; j < 2; ++j) {
        GLDS(vs + j * 4096 + voff, dst + 8192 + j * 4096, 0); GLDS(vs + j * 4096 + voff, dst + 8192 + j * 4096, 1024);
        GLDS(vs + j * 4096 + voff, dst + 8192 + j * 4096, 2048); GLDS(vs + j * 4096 + voff, dst + 8192 + j * 4096, 3072);
      }
      ++groups;
      ++fill;
    };
#pragma unroll 1
    for (int i = 0; i < NS && ok; ++i) {
      room();
      if (!ok) break;
      if (DYN) ctl[W_PROG] = (unsigned)(i + 1);
      if (i == 24) stamp(7);
      kv_fill((unsigned)(split * NS + i));
    }
    if constexpr (DYN) {
#pragma unroll 1
      for (int j = 0; ok; ++j) {
        unsigned t = rfl(ctl[W_TKT + (j & 7)]);
        if ((t >> 16) != (unsigned)(j + 1)) {
          drain();
          ok = spin([&] { t = rfl(ctl[W_TKT + (j & 7)]); return (t >> 16) == (unsigned)(j + 1); }, false);
          if (!ok) break;
        }
        t &= 0xffffu;
        if (t >= (unsigned)NDYN) break;
        room();
        if (!ok) break;
        ctl[W_PROG] = (unsigned)(NS + j + 1);
        kv_fill(16u * NS + t);
      }
      ctl[W_END] = (unsigned)fill;
    }
    stamp(0);
    sv[2] = (u64)fill;
    if (WOLAST) {
#pragma unroll 1
      for (int i = 0; i < 4 && ok; ++i) {
        room();
        if (!ok) break;
        weight_fill(wo + (size_t)i * 32 * 2048, wo_off, 2 * 2048, true);
      }
    }
    drain();
    stamp(1);
    flush(8, 8);
    return;
  }

  const __amdgpu_buffer_rsrc_t rs = __builtin_amdgcn_make_buffer_rsrc(p.ws, 0, WS_END, 0x00020000);
  auto ld = [&](int off) -> v4u { return __builtin_amdgcn_raw_buffer_load_b128(rs, off, 0, 16); };
  auto st = [&](v4u v, int off) { __builtin_amdgcn_raw_buffer_store_b128(v, rs, off, 0, 16); };
  auto lds_add = [&](int w) { if (lane == 0) __hip_atomic_fetch_add((LAS unsigned*)&ctl[w], 1u, __ATOMIC_RELAXED, __HIP_MEMORY_SCOPE_WORKGROUP); };
  auto gather = [&](auto& v, auto& off, unsigned tA, unsigned tB, bool nap) -> bool {
    constexpr int N = sizeof(v) / sizeof(v[0]);
#pragma unroll
    for (int j = 0; j < N; ++j) v[j] = v2u{0u, 0u};
    return spin([&] {
      v4u t[N];
#pragma unroll
      for (int j = 0; j < N; ++j) t[j] = ld(off[j]);
      __builtin_amdgcn_sched_barrier(0);
      bool pend = false;
#pragma unroll
      for (int j = 0; j < N; ++j) {
        const bool hit = (t[j][1] == tA) & (t[j][3] == tB) & (off[j] != OOB);
        v[j][0] = hit ? t[j][0] : v[j][0];
        v[j][1] = hit ? t[j][2] : v[j][1];
        off[j] = hit ? OOB : off[j];
        pend = pend | (off[j] != OOB);
      }
      return __any(pend) == 0;
    }, nap);
  };

  if (wave == NCONS + 1) {
    const int tkoff = WS_TICKET + 256 * (bid >> 4);
    if (lane == 0) __hip_atomic_store((GAS u64*)(p.ws + WS_STAMP) + 8 + bid, t_start, __ATOMIC_RELAXED, __HIP_MEMORY_SCOPE_AGENT);
    __builtin_amdgcn_s_barrier();
    if (DYN) __builtin_amdgcn_raw_buffer_store_b32(0u, rs, (split == 0 && lane == 0) ? tkoff : OOB, 0, 16);
    const u64 ep = __hip_atomic_load((GAS u64*)(p.ws + WS_EPOCH + 256 * br), __ATOMIC_RELAXED, __HIP_MEMORY_SCOPE_AGENT);
    const unsigned tA = rfl((unsigned)ep) + 1u, tB = rfl((unsigned)(ep >> 32)) + 1u;
    vmwait<0>();
    ctl[W_TAGA] = tA; ctl[W_TAGB] = tB;
    LDS_WAIT();
    lds_add(W_QCNT);
    if (!spin([&] { return rfl(ctl[W_QPUB]) != 0u; }, true)) return;
    {
      v2u qv[4];
      int off[4];
#pragma unroll
      for (int j = 0; j < 4; ++j) off[j] = WS_Q + (grp * 2 + b) * 4096 + lane * 16 + 1024 * j;
      if (!gather(qv, off, tA, tB, true)) return;
#pragma unroll
      for (int j = 0; j < 4; ++j) *(LAS v2u*)(L + L_QIMG + j * 512 + lane * 8) = qv[j];
      LDS_WAIT();
      ctl[W_QRDY] = 1u;
    }
    stamp(0);
    if constexpr (DYN) {
      constexpr int AHEAD = 4;
      const int myoff = lane == 0 ? tkoff : OOB;
      auto claim = [&]() -> unsigned { return (unsigned)__builtin_amdgcn_raw_ptr_buffer_atomic_add_i32(1, rs, myoff, 0, 0); };
      auto gate = [&](int j) { return spin([&] { return (int)rfl(ctl[W_PROG]) + AHEAD >= NS + j + 1; }, true); };
      auto post = [&](int j, unsigned t) -> bool {
        t = rfl(t);
        const unsigned tt = t < (unsigned)NDYN ? t : 0xffffu;
        ctl[W_TKT + (j & 7)] = ((unsigned)(j + 1) << 16) | tt;
        return t < (unsigned)NDYN;
      };
      if (!gate(0)) return;
      unsigned t0 = claim();
      if (!gate(1)) return;
      unsigned t1 = claim();
#pragma unroll 1
      for (int j = 0; j <= NDYN; j += 2) {
        if (!post(j, t0)) break;
        if (!gate(j + 2)) return;
        t0 = claim();
        if (!post(j + 1, t1)) break;
        if (!gate(j + 3)) return;
        t1 = claim();
      }
    }
    if (!spin([&] { return rfl(ctl[W_ORDY]) >= (unsigned)NCONS; }, true)) return;
    stamp(1);
    v2u r[4];
    int off[4];
#pragma unroll
    for (int j = 0; j < 4; ++j) off[j] = WS_H2 + bid * 4096 + lane * 16 + 1024 * j;
    if (!gather(r, off, tA, tB, false)) return;
    float s0 = (f(r[0][0]) + f(r[1][0])) + (f(r[2][0]) + f(r[3][0])), s1 = (f(r[0][1]) + f(r[1][1])) + (f(r[2][1]) + f(r[3][1]));
    s0 += dpp<ROR8>(s0); s1 += dpp<ROR8>(s1);
    s0 = xrow_sum(s0); s1 = xrow_sum(s1);
    if (lane < 8) {
      const int oc = bid & 127;
      const int col = 64 * (oc >> 3) + 16 * (lane >> 1) + 4 * ((oc >> 1) & 3) + 2 * (oc & 1);
      *(GAS unsigned*)(p.out + (lane & 1) * 2048 + br * 1024 + col) = cvtpk(s0, s1);
    }
    if ((bid & 127) == 0 && lane == 0)
      __hip_atomic_store((GAS u64*)(p.ws + WS_EPOCH + 256 * br), ((u64)tB << 32) | tA, __ATOMIC_RELAXED, __HIP_MEMORY_SCOPE_AGENT);
    if (EXP == 0) { while (rt() - t_start < 10000ull) __builtin_amdgcn_s_sleep(2); }
    if (EXP == 1) {
      const u64 a = __hip_atomic_load((GAS u64*)(p.ws + WS_STAMP), __ATOMIC_RELAXED, __HIP_MEMORY_SCOPE_AGENT);
      if (t_start - a < 5000ull) { while (rt() - a < 10000ull) __builtin_amdgcn_s_sleep(2); }
    }
    if (EXP == 4) {
      const u64 a = __hip_atomic_load((GAS u64*)(p.ws + WS_STAMP), __ATOMIC_RELAXED, __HIP_MEMORY_SCOPE_AGENT);
      if (t_start - a < 5000ull) { while (rt() - a < 10000ull + 4 * (t_start - a)) __builtin_amdgcn_s_sleep(2); }
    }
    if (EXP == 3) {
      while (rt() - t_start < 7000ull) __builtin_amdgcn_s_sleep(2);
      u64 mn = t_start;
      for (int i = 0; i < 4; ++i) { const u64 a = __hip_atomic_load((GAS u64*)(p.ws + WS_STAMP) + 8 + 64 * i + lane, __ATOMIC_RELAXED, __HIP_MEMORY_SCOPE_AGENT); if (t_start - a < 3000ull && a < mn) mn = a; }
      for (int o = 32; o >= 1; o >>= 1) { const u64 y = __shfl_xor(mn, o); mn = y < mn ? y : mn; }
      while (rt() - mn < 10000ull) __builtin_amdgcn_s_sleep(2);
    }
    return;
  }

  const int ci = wave - 1;
  const int tq = (lane >> 2) & 3, tp = lane & 3;
  v4u xf[8];
#pragma unroll
  for (int ks = 0; ks < 8; ++ks) xf[ks] = *(const GAS v4u*)(p.x[br] + (c & 1) * 1024 + ci * 256 + ks * 32 + 8 * g);
  __builtin_amdgcn_s_barrier();
  stamp(1);
  auto wait_full = [&](int fl) -> bool {
    const bool r = spin([&] { return rfl(ctl[W_FULL + (fl & (NSLOT - 1))]) >= (unsigned)(fl + 1); }, true);
    __builtin_amdgcn_fence(__ATOMIC_ACQUIRE, "workgroup");
    return r;
  };
  const v4u zero4 = {0u, 0u, 0u, 0u};

  {
    if (!wait_full(ci)) return;
    const LAS unsigned char* s = L + L_RING + ci * SLOT + (8 * g + tq) * 64 + tp * 8;
    v4u w0[8], w1[8];
#pragma unroll
    for (int ks = 0; ks < 8; ++ks) { w0[ks] = trfrag(s + ks * 2048, s + ks * 2048 + 256); w1[ks] = trfrag(s + ks * 2048 + 32, s + ks * 2048 + 288); }
    LDS_WAIT();
    ctl[W_FREE + ci] = (unsigned)(ci + 1);
    PIN8(w0); PIN8(w1);
    f32x4 a0 = {0.f, 0.f, 0.f, 0.f}, a1 = {0.f, 0.f, 0.f, 0.f};
#pragma unroll
    for (int ks = 0; ks < 8; ++ks) { a0 = mfma(w0[ks], xf[ks], a0); a1 = mfma(w1[ks], xf[ks], a1); }
    if (c < 2) {
      LAS unsigned char* d = L + L_QRED + ci * 256 + c * 128 + g * 16;
      *(LAS f32x4*)d = a0; *(LAS f32x4*)(d + 64) = a1;
    }
    LDS_WAIT();
    lds_add(W_QCNT);
  }
  stamp(2);
  if (!spin([&] { return rfl(ctl[W_QCNT]) >= (unsigned)(NCONS + 1); }, false)) return;
  __builtin_amdgcn_fence(__ATOMIC_ACQUIRE, "workgroup");
  const unsigned tA = rfl(ctl[W_TAGA]), tB = rfl(ctl[W_TAGB]);
  if (ci == 0) {
    f32x4 a = *(const LAS f32x4*)(L + L_QRED + c * 16);
#pragma unroll
    for (int w = 1; w < NCONS; ++w) a += *(const LAS f32x4*)(L + L_QRED + w * 256 + c * 16);
    st(v4u{cvtpk(a[0], a[1]), tA, cvtpk(a[2], a[3]), tB}, lane < 16 ? WS_Q + (grp * 2 + (c >> 3)) * 4096 + qslice * 128 + (c & 7) * 16 : OOB);
    ctl[W_QPUB] = 1u;
  }
  stamp(3);
  if (!spin([&] { return rfl(ctl[W_QRDY]) != 0u; }, true)) return;
  __builtin_amdgcn_fence(__ATOMIC_ACQUIRE, "workgroup");
  v4u qf[4];
#pragma unroll
  for (int s = 0; s < 4; ++s) {
    const v4u t = *(const LAS v4u*)(L + L_QIMG + (c & 7) * 256 + s * 64 + g * 16);
    qf[s] = c < 8 ? t : zero4;
  }
  stamp(4);

  f32x4 acc[8];
#pragma unroll
  for (int e = 0; e < 8; ++e) acc[e] = f32x4{0.f, 0.f, 0.f, 0.f};
  float m = -INFINITY, l = 0.f;
  unsigned kra[4];
#pragma unroll
  for (int s = 0; s < 4; ++s) kra[s] = (unsigned)c * 256u + (unsigned)(((4 * s + g) ^ (c & SWZ)) * 16);
  const unsigned vra = 8192u + (unsigned)g * 1024u + (unsigned)c * 16u;
  int fl = KV0 + ci;
#pragma unroll 1
  for (;; fl += NCONS) {
    bool done = false;
    if (!spin([&] {
          const unsigned fu = rfl(ctl[W_FULL + (fl & (NSLOT - 1))]);
          const unsigned en = DYN ? rfl(ctl[W_END]) : (unsigned)(KV0 + NS);
          done = (unsigned)fl >= en;
          return done || fu >= (unsigned)(fl + 1);
        }, true)) return;
    if (done) break;
    __builtin_amdgcn_fence(__ATOMIC_ACQUIRE, "workgroup");
    const LAS unsigned char* s = L + L_RING + (fl & (NSLOT - 1)) * SLOT;
    v4u K[8], V[8];
#pragma unroll
    for (int j = 0; j < 8; ++j) K[j] = *(const LAS v4u*)(s + kra[j & 3] + (j >> 2) * 4096);
#pragma unroll
    for (int j = 0; j < 8; ++j) V[j] = *(const LAS v4u*)(s + vra + (j >> 2) * 4096 + (j & 3) * 256);
    LDS_WAIT();
    ctl[W_FREE + (fl & (NSLOT - 1))] = (unsigned)(fl + 1);
    PIN8(K); PIN8(V);
    f32x4 s0 = {0.f, 0.f, 0.f, 0.f}, s1 = {0.f, 0.f, 0.f, 0.f};
#pragma unroll
    for (int j = 0; j < 4; ++j) { s0 = mfma(K[j], qf[j], s0); s1 = mfma(K[4 + j], qf[j], s1); }
    float tm = fmaxf(fmaxf(fmaxf(s0[0], s0[1]), fmaxf(s0[2], s0[3])), fmaxf(fmaxf(s1[0], s1[1]), fmaxf(s1[2], s1[3])));
    tm = xrow_max(tm) * C2;
    const float mn = fmaxf(m, tm);
    const float alpha = __builtin_amdgcn_exp2f(m - mn);
    m = mn;
    float pr[8];
#pragma unroll
    for (int i = 0; i < 4; ++i) { pr[i] = __builtin_amdgcn_exp2f(fmaf(s0[i], C2, -mn)); pr[4 + i] = __builtin_amdgcn_exp2f(fmaf(s1[i], C2, -mn)); }
    l = l * alpha + (((pr[0] + pr[1]) + (pr[2] + pr[3])) + ((pr[4] + pr[5]) + (pr[6] + pr[7])));
    const v4u pf = {cvtpk(pr[0], pr[1]), cvtpk(pr[2], pr[3]), cvtpk(pr[4], pr[5]), cvtpk(pr[6], pr[7])};
#pragma unroll
    for (int e = 0; e < 8; ++e) acc[e] = mfma(tfrag(V, e), pf, acc[e] * alpha);
  }

  stamp(5);
  l = xrow_sum(l);
  if (c < 8) {
    LAS unsigned char* row = L + L_MRG + (ci * 8 + c) * MROW + g * 128;
#pragma unroll
    for (int r = 0; r < 4; ++r) {
      *(LAS f32x4*)(row + r * 32) = f32x4{acc[0][r], acc[1][r], acc[2][r], acc[3][r]};
      *(LAS f32x4*)(row + r * 32 + 16) = f32x4{acc[4][r], acc[5][r], acc[6][r], acc[7][r]};
    }
    if (g == 0) { LAS float* mm = (LAS float*)(L + L_MML) + (ci * 8 + c) * 2; mm[0] = m; mm[1] = l; }
  }
  LDS_WAIT();
  lds_add(W_MCNT);
  if (!spin([&] { return rfl(ctl[W_MCNT]) >= (unsigned)NCONS; }, false)) return;
  __builtin_amdgcn_fence(__ATOMIC_ACQUIRE, "workgroup");
  {
    const int h = 2 * ci + (lane >> 5), l5 = lane & 31;
    float mw[NCONS], lw[NCONS];
    f32x4 aw[NCONS];
#pragma unroll
    for (int w = 0; w < NCONS; ++w) {
      const LAS float* mm = (const LAS float*)(L + L_MML) + (w * 8 + h) * 2;
      mw[w] = mm[0]; lw[w] = mm[1];
      aw[w] = *(const LAS f32x4*)(L + L_MRG + (w * 8 + h) * MROW + l5 * 16);
    }
    const float M = fmaxf(fmaxf(mw[0], mw[1]), fmaxf(mw[2], mw[3]));
    float Ls = 0.f;
    f32x4 o = {0.f, 0.f, 0.f, 0.f};
#pragma unroll
    for (int w = 0; w < NCONS; ++w) { const float wt = __builtin_amdgcn_exp2f(mw[w] - M); Ls = fmaf(wt, lw[w], Ls); o += aw[w] * wt; }
    const int rec = WS_H1 + ((grp * 32 + h * 4 + (l5 >> 3)) * 32 + gi) * 256 + (l5 & 7) * 16;
    st(v4u{u(o[0]), tA, u(o[1]), tB}, rec);
    st(v4u{u(o[2]), tA, u(o[3]), tB}, rec + 128);
    st(v4u{u(M), tA, u(Ls), tB}, l5 == 0 ? WS_ML + ((grp * 8 + h) * 32 + gi) * 16 : OOB);
  }
  stamp(6);

  {
    const int pb = c & 1, sp = c >> 1;
    v2u d[10];
    int off[10];
    const int rec = WS_H1 + ((grp * 32 + (gi & 7) * 4 + ci) * 32 + pb * 16 + 2 * sp) * 256 + g * 16;
    const int mlo = WS_ML + ((grp * 8 + (gi & 7)) * 32 + pb * 16 + 2 * sp) * 16;
#pragma unroll
    for (int j = 0; j < 10; ++j) off[j] = j < 8 ? rec + 64 * j : mlo + 16 * (j - 8);
    if (!gather(d, off, tA, tB, false)) return;
    float M = fmaxf(f(d[8][0]), f(d[9][0]));
    M = fmaxf(M, dpp<ROR2>(M)); M = fmaxf(M, dpp<ROR4>(M)); M = fmaxf(M, dpp<ROR8>(M));
    const float w0 = __builtin_amdgcn_exp2f(f(d[8][0]) - M), w1 = __builtin_amdgcn_exp2f(f(d[9][0]) - M);
    float Ls = w0 * f(d[8][1]) + w1 * f(d[9][1]);
    Ls += dpp<ROR2>(Ls); Ls += dpp<ROR4>(Ls); Ls += dpp<ROR8>(Ls);
    const float inv = 1.0f / Ls;
    v4u of;
#pragma unroll
    for (int j = 0; j < 4; ++j) {
      float o0 = w0 * f(d[j][0]) + w1 * f(d[4 + j][0]), o1 = w0 * f(d[j][1]) + w1 * f(d[4 + j][1]);
      o0 += dpp<ROR2>(o0); o1 += dpp<ROR2>(o1); o0 += dpp<ROR4>(o0); o1 += dpp<ROR4>(o1); o0 += dpp<ROR8>(o0); o1 += dpp<ROR8>(o1);
      of[j] = cvtpk(o0 * inv, o1 * inv);
    }
    *(LAS v4u*)(L + L_OFRAG + ci * 1024 + lane * 16) = of;
    LDS_WAIT();
    lds_add(W_ORDY);
  }
  stamp(1);

  const int wo0 = DYN ? (int)rfl(ctl[W_END]) : KV0 + NS;
  if (!wait_full(wo0 + 3)) return;
  v4u wf[4][4];
#pragma unroll
  for (int ks = 0; ks < 4; ++ks) {
    const LAS unsigned char* s = L + L_RING + ((wo0 + ks) & (NSLOT - 1)) * SLOT + (16 * (tq >> 1) + 4 * g + (tq & 1)) * 512 + ci * 128 + tp * 8;
#pragma unroll
    for (int e = 0; e < 4; ++e) wf[ks][e] = trfrag(s + e * 32, s + e * 32 + 1024);
  }
  if (!spin([&] { return rfl(ctl[W_ORDY]) >= (unsigned)NCONS; }, false)) return;
  __builtin_amdgcn_fence(__ATOMIC_ACQUIRE, "workgroup");
  f32x4 a0 = {0.f, 0.f, 0.f, 0.f}, a1 = {0.f, 0.f, 0.f, 0.f};
#pragma unroll
  for (int ks = 0; ks < 4; ++ks) {
    const v4u of = *(const LAS v4u*)(L + L_OFRAG + ks * 1024 + lane * 16);
#pragma unroll
    for (int e = 0; e < 4; e += 2) {
      a0 = mfma(wf[ks][e], (c >> 1) == e ? of : zero4, a0);
      a1 = mfma(wf[ks][e + 1], (c >> 1) == e + 1 ? of : zero4, a1);
    }
  }
  a0 += a1;
  {
    const int o0 = c < 8 ? WS_H2 + (br * 128 + (((gi >> 3) * 4 + ci) * 4 + g) * 2) * 4096 + (kvh * 8 + (gi & 7)) * 128 + c * 16 : OOB - 4096;
    st(v4u{u(a0[0]), tA, u(a0[1]), tB}, o0);
    st(v4u{u(a0[2]), tA, u(a0[3]), tB}, o0 + 4096);
  }
  stamp(7);
  sv[0] = t_start;
  if (ci == 0) flush(0, 8);
}

__global__ void k_pre(unsigned char* ws) { if (threadIdx.x == 0) *(u64*)(ws + WS_STAMP) = rt(); }
__global__ void k_post(unsigned char* ws) {
  const u64 now = rt();
  u64 mx = 0;
  for (int i = 0; i < 4; ++i) { const u64 a = ((u64*)(ws + WS_STAMP))[8 + 64 * i + threadIdx.x]; if (now - a < 20000ull && a > mx) mx = a; }
  for (int o = 32; o >= 1; o >>= 1) { const u64 y = __shfl_xor(mx, o); mx = y > mx ? y : mx; }
  if (mx) while (rt() - mx < 10000ull) __builtin_amdgcn_s_sleep(2);
}
template <int WIN, int DYN, int NSTATIC, int WOLAST, int STAMPS>
static void launch(void* const* d_in, void* d_out, void* d_ws, hipStream_t stream) {
  Params p;
  for (int br = 0; br < 2; ++br) {
    p.x[br] = (const u16*)d_in[5 * br + 0]; p.k[br] = (const u16*)d_in[5 * br + 1]; p.v[br] = (const u16*)d_in[5 * br + 2];
    p.wq[br] = (const u16*)d_in[5 * br + 3]; p.wo[br] = (const u16*)d_in[5 * br + 4];
  }
  p.out = (u16*)d_out; p.ws = (unsigned char*)d_ws;
  (void)hipFuncSetAttribute((const void*)k_fused<WIN, DYN, NSTATIC, WOLAST, STAMPS>, hipFuncAttributeMaxDynamicSharedMemorySize, L_BYTES);
  if (EXP == 1 || EXP == 4) k_pre<<<1, 64, 0, stream>>>(p.ws);
  hipLaunchKernelGGL((k_fused<WIN, DYN, NSTATIC, WOLAST, STAMPS>), dim3(NWG), dim3(NTHR), L_BYTES, stream, p);
  if (EXP == 2) k_post<<<1, 64, 0, stream>>>(p.ws);
}

}

extern "C" void kernel_launch(void* const* d_in, const int* in_sizes, int n_in, void* d_out, int out_size, void* d_ws, size_t ws_size,
                              hipStream_t stream) {
  fused::launch<CFG_WIN, CFG_DYN, CFG_NSTATIC, CFG_WOLAST, CFG_STAMPS>(d_in, d_out, d_ws, stream);
}
```

```cpp
#include <hip/hip_runtime.h>
#include <hip/hip_bf16.h>
#include <cstdint>

#define GAS __attribute__((address_space(1)))
#define LAS __attribute__((address_space(3)))
typedef unsigned v4u __attribute__((ext_vector_type(4)));
typedef unsigned v2u __attribute__((ext_vector_type(2)));
typedef float f32x4 __attribute__((ext_vector_type(4)));
typedef short bf16x8 __attribute__((ext_vector_type(8)));
typedef short s16x4 __attribute__((ext_vector_type(4)));
typedef unsigned short u16;
typedef unsigned long long u64;

#define CFG 24, 0, 100, 0, 0

#define QROT 1
namespace fused {

constexpr int SK = 32768, DH = 128;
constexpr int NWG = 256, NCONS = 4;
constexpr int NSLOT = 8, SLOT = 16384;
constexpr int TILES = SK / 32;
constexpr float C2 = 0.088388347648318440f * 1.4426950408889634f;
constexpr u64 TCAP = 2000000;
constexpr int OOB = 0x7ffffff0;

constexpr int WS_EPOCH = 0;
constexpr int WS_RATE = 4096;
constexpr int WS_Q = 65536;
constexpr int WS_H2 = WS_Q + 8 * 2 * 16384;
constexpr int WS_H1 = WS_H2 + 256 * 4096;
constexpr int WS_ML = WS_H1 + 256 * 32 * 256;
constexpr int WS_END = WS_ML + 64 * 32 * 128;
constexpr int WS_STAMP = WS_END;

constexpr int L_RING = 0;
constexpr int L_CTL = NSLOT * SLOT;
constexpr int L_QIMG = L_CTL + 256;
constexpr int L_QRED = L_QIMG + 2048;
constexpr int L_OFRAG = L_QRED + 2048;
constexpr int MROW = 528;
constexpr int L_MRG = L_OFRAG + 512;
constexpr int L_MML = L_MRG + 4 * 8 * MROW;
constexpr int L_BYTES = L_MML + 256;
enum { W_FULL = 0, W_FREE = 16, W_ABORT = 32, W_END, W_QCNT, W_QRDY, W_MCNT, W_ORDY, W_TAGA, W_TAGB, W_QPUB, W_T1, W_T2, W_TOK, W_DEAL, W_WCNT };
static_assert(L_BYTES <= 160 * 1024 && L_BYTES > 80 * 1024, "one workgroup per CU");

struct Params { const u16* x[2]; const u16* k[2]; const u16* v[2]; const u16* wq[2]; const u16* wo[2]; u16* out; unsigned char* ws; };

__device__ __forceinline__ u64 rt() { return __builtin_amdgcn_s_memrealtime(); }
__device__ __forceinline__ unsigned rfl(unsigned v) { return (unsigned)__builtin_amdgcn_readfirstlane((int)v); }
__device__ __forceinline__ unsigned cvtpk(float lo, float hi) {
  unsigned r; asm("v_cvt_pk_bf16_f32 %0, %1, %2" : "=v"(r) : "v"(lo), "v"(hi)); return r;
}
__device__ __forceinline__ f32x4 mfma(v4u a, v4u b, f32x4 c) {
  return __builtin_amdgcn_mfma_f32_16x16x32_bf16(__builtin_bit_cast(bf16x8, a), __builtin_bit_cast(bf16x8, b), c, 0, 0, 0);
}
__device__ __forceinline__ float f(unsigned u) { return __uint_as_float(u); }
__device__ __forceinline__ unsigned u(float x) { return __float_as_uint(x); }
__device__ __forceinline__ float xrow_max(float x) {
  auto s = __builtin_amdgcn_permlane16_swap(u(x), u(x), false, false);
  x = fmaxf(f(s[0]), f(s[1]));
  auto t = __builtin_amdgcn_permlane32_swap(u(x), u(x), false, false);
  return fmaxf(f(t[0]), f(t[1]));
}
__device__ __forceinline__ float xrow_sum(float x) {
  auto s = __builtin_amdgcn_permlane16_swap(u(x), u(x), false, false);
  x = f(s[0]) + f(s[1]);
  auto t = __builtin_amdgcn_permlane32_swap(u(x), u(x), false, false);
  return f(t[0]) + f(t[1]);
}
template <int CTRL> __device__ __forceinline__ float dpp(float x) {
  return __builtin_bit_cast(float, __builtin_amdgcn_mov_dpp(__builtin_bit_cast(int, x), CTRL, 0xf, 0xf, true));
}
constexpr int ROR1 = 0x121, ROR2 = 0x122, ROR4 = 0x124, ROR8 = 0x128, SHR1 = 0x111, SHR2 = 0x112, SHR4 = 0x114, SHR8 = 0x118;
template <int N> struct Nap { static constexpr int value = N; };
template <int N> __device__ __forceinline__ void vmwait() { asm volatile("s_waitcnt vmcnt(%0)" ::"i"(N) : "memory"); }
#define LDS_WAIT() asm volatile("s_waitcnt lgkmcnt(0)" ::: "memory")
#define PIN8(w) asm volatile("" : "+v"(w[0]), "+v"(w[1]), "+v"(w[2]), "+v"(w[3]), "+v"(w[4]), "+v"(w[5]), "+v"(w[6]), "+v"(w[7]) : : "memory")
#define GLDS(src, dst, imm) __builtin_amdgcn_global_load_lds((const GAS unsigned*)(src), (LAS unsigned*)(dst), 16, (imm), 2)

__device__ __forceinline__ v4u trfrag(const LAS unsigned char* lo, const LAS unsigned char* hi) {
  const v2u a = __builtin_bit_cast(v2u, __builtin_amdgcn_ds_read_tr16_b64_v4i16((LAS s16x4*)lo));
  const v2u b = __builtin_bit_cast(v2u, __builtin_amdgcn_ds_read_tr16_b64_v4i16((LAS s16x4*)hi));
  return v4u{a[0], a[1], b[0], b[1]};
}

template <int WIN, int MLF, int QDELAY, int WOE, int STAMPS>
__global__ void __launch_bounds__(64 * (NCONS + 2)) k_fused(Params p) {
  constexpr int BAL = 0, NSTATIC = 48, DRY = 0, GRP = 8, NAP1 = 0, MLS = MLF ? 128 : 16;
  static_assert(WIN % GRP == 0 && WIN >= 8 && WIN <= 56 && (GRP == 4 || GRP == 8), "window in 1 KiB pieces");
  extern __shared__ __attribute__((aligned(16))) unsigned char lds_raw[];
  LAS unsigned char* L = (LAS unsigned char*)lds_raw;
  volatile LAS unsigned* ctl = (volatile LAS unsigned*)(L + L_CTL);
  const u64 t_start = rt();
  const int bid = blockIdx.x, br = bid >> 7, grp = bid >> 5, kvh = grp & 3, gi = bid & 31, b = gi >> 4, split = bid & 15;
  const int tid = threadIdx.x, lane = tid & 63, wave = (int)rfl(tid >> 6);
  const int c = lane & 15, g = lane >> 4;
  if (__builtin_expect(wave >= 1 && wave <= NCONS, 1)) {
    const unsigned char* wq = (const unsigned char*)p.wq[br] + (size_t)(b * 512 + (wave - 1) * 128) * 8192 + kvh * 2048 + split * 128;
    const unsigned wq_off = (unsigned)(lane >> 3) * 8192u + (unsigned)(lane & 7) * 16u;
#pragma unroll
    for (int j = 0; j < 16; ++j) GLDS(wq + (size_t)j * 8 * 8192 + wq_off, L + L_RING + (wave - 1) * SLOT + j * 1024, 0);
  }
  constexpr int NS = BAL ? NSTATIC : TILES / 16;
  constexpr int POOL = TILES - 16 * NS;
  constexpr int P1 = 10, P2 = NS - 12;
  constexpr int KV0 = 4 + WOE;
  constexpr unsigned END_UNKNOWN = 0x7fffffffu;
  const bool dry = DRY && wave >= NCONS + 2;
  u64* stamps = (u64*)(p.ws + WS_STAMP) + bid * 32;
  u64 sv[8] = {};
  auto stamp = [&](int i) { if constexpr (STAMPS) sv[i] = rt(); };
  auto flush = [&](int base, int n) { if constexpr (STAMPS) { if (lane == 0) for (int i = 0; i < n; ++i) stamps[base + i] = sv[i]; } };

  auto spin = [&](auto&& cond, auto nap) -> bool {
    for (unsigned it = 1;; ++it) {
      if (cond() || dry) return true;
      if constexpr (decltype(nap)::value > 0) __builtin_amdgcn_s_sleep(decltype(nap)::value);
      if ((it & 255u) == 0u) {
        if (rfl(ctl[W_ABORT])) return false;
        if (rt() - t_start > TCAP) { ctl[W_ABORT] = 1u; return false; }
      }
    }
  };

  if (__builtin_expect(wave == 0, 1)) {
    __builtin_amdgcn_s_setprio(3);
    ctl[lane] = (lane == W_END) ? (BAL ? END_UNKNOWN : (unsigned)(KV0 + NS)) : 0u;
    const size_t sbase = (size_t)(b * 4 + kvh) * SK * DH * 2;
    const unsigned char* kb = (const unsigned char*)p.k[br] + sbase;
    const unsigned char* vb = (const unsigned char*)p.v[br] + sbase;
    const unsigned char* wo = (const unsigned char*)p.wo[br] + (size_t)((kvh * 8 + (gi & 7)) * 128) * 2048 + (gi >> 3) * 512;
    const unsigned wo_off = (unsigned)(lane >> 5) * 2048u + (unsigned)(lane & 31) * 16u;
    unsigned koff[4], voff[2];
#pragma unroll
    for (int j = 0; j < 4; ++j) koff[j] = (unsigned)g * 256u + (unsigned)((c ^ (4 * j + g)) * 16);
#pragma unroll
    for (int j = 0; j < 2; ++j) voff[j] = (unsigned)g * 256u + (unsigned)((c ^ (2 * (4 * j + g))) * 16);
    int pieces = 64, pub = 4, fill = 4;
    bool ok = true;
    auto pace = [&]() {
      vmwait<WIN - GRP>();
      if (16 * pub + 16 <= pieces - (WIN - GRP)) { ctl[W_FULL + (pub & (NSLOT - 1))] = (unsigned)(pub + 1); ++pub; }
      pieces += GRP;
    };
    auto drain = [&]() { vmwait<0>(); for (; pub < fill; ++pub) ctl[W_FULL + (pub & (NSLOT - 1))] = (unsigned)(pub + 1); };
    auto park = [&](auto&& cond) {
      if (!cond()) {
        u64 tp = 0;
        if constexpr (STAMPS) tp = rt();
        drain();
        ok = ok && spin(cond, Nap<1>{});
        if constexpr (STAMPS) { sv[5] += rt() - tp; sv[6] += 100; }
      }
      __builtin_amdgcn_fence(__ATOMIC_ACQUIRE, "workgroup");
    };
    auto room = [&]() {
      if (fill >= NSLOT) park([&] { return rfl(ctl[W_FREE + (fill & (NSLOT - 1))]) >= (unsigned)(fill - NSLOT + 1); });
    };
    auto wo_fills = [&](int h0, int h1) {
#pragma unroll 1
      for (int i = h0; i < h1 && ok; ++i) {
        if ((i & 1) == 0) room();
        if (!ok) break;
        LAS unsigned char* dst = L + L_RING + (fill & (NSLOT - 1)) * SLOT + (i & 1) * 8192;
        const unsigned char* src = wo + (size_t)i * 16 * 2048 + wo_off;
#pragma unroll
        for (int j = 0; j < 8; ++j) {
          if (j % GRP == 0) pace();
          GLDS(src + j * 4096, dst + j * 1024, 0);
        }
        fill += i & 1;
      }
    };
    LDS_WAIT();
    __builtin_amdgcn_s_barrier();
    stamp(3);
    if (WOE > 0) wo_fills(0, 2 * WOE);
    int ntile = NS;
    u64 tl[6] = {};
    unsigned tile = (unsigned)(split * NS);
#pragma unroll 1
    for (int i = 0; i < ntile && ok; ++i, ++tile) {
      if constexpr (BAL) {
        if (i == P1) ctl[W_T1] = (unsigned)rt();
        if (i == P2) { ctl[W_T2] = (unsigned)rt(); ctl[W_TOK] = 1u; }
      }
      room();
      if (!ok) break;
      if (i == 24) stamp(7);
      if constexpr (STAMPS) { if (i == 8) tl[0] = rt(); if (i == 16) tl[1] = rt(); if (i == 32) tl[2] = rt(); if (i == 40) tl[3] = rt(); if (i == 48) tl[4] = rt(); if (i == 56) tl[5] = rt(); }
      LAS unsigned char* dst = L + L_RING + (fill & (NSLOT - 1)) * SLOT;
      const unsigned char* ks = kb + (size_t)tile * 8192;
      const unsigned char* vs = vb + (size_t)tile * 8192;
#pragma unroll
      for (int j = 0; j < 2; ++j) {
        if (GRP == 4 || j == 0) pace();
        GLDS(ks + j * 4096 + koff[0], dst + j * 4096, 0); GLDS(ks + j * 4096 + koff[1], dst + j * 4096, 1024);
        GLDS(ks + j * 4096 + koff[2], dst + j * 4096, 2048); GLDS(ks + j * 4096 + koff[3], dst + j * 4096, 3072);
      }
#pragma unroll
      for (int j = 0; j < 2; ++j) {
        if (GRP == 4 || j == 0) pace();
        GLDS(vs + j * 4096 + voff[0], dst + 8192 + j * 4096, 0); GLDS(vs + j * 4096 + voff[1], dst + 8192 + j * 4096, 1024);
        GLDS(vs + j * 4096 + voff[0], dst + 8192 + j * 4096, 2048); GLDS(vs + j * 4096 + voff[1], dst + 8192 + j * 4096, 3072);
      }
      ++fill;
      if constexpr (BAL) {
        if (i == NS - 1) {
          unsigned deal = 0u;
          park([&] { deal = rfl(ctl[W_DEAL]); return deal != 0u; });
          ntile = NS + (int)((deal >> 12) & 0xfffu);
          tile = 16u * NS + (deal & 0xfffu) - 1u;
        }
      }
    }
    stamp(0);
    sv[2] = (u64)fill;
    wo_fills(2 * WOE, 8);
    drain();
    stamp(1);
    flush(8, 8);
    if constexpr (STAMPS) { if (lane == 0) { stamps[24] = (unsigned)__builtin_amdgcn_s_getreg(20 | (3 << 11)) & 0xfu; for (int i = 0; i < 6; ++i) stamps[25 + i] = tl[i]; } }
    return;
  }

  const __amdgpu_buffer_rsrc_t rs = __builtin_amdgcn_make_buffer_rsrc(p.ws, 0, WS_END, 0x00020000);
  auto ld = [&](int off) -> v4u { return __builtin_amdgcn_raw_buffer_load_b128(rs, off, 0, 16); };
  auto st = [&](v4u v, int off) { __builtin_amdgcn_raw_buffer_store_b128(v, rs, dry ? OOB : off, 0, 16); };
  auto put = [&](int w, unsigned v) { if (!dry) ctl[w] = v; };
  auto lds_add = [&](int w) { if (lane == 0 && !dry) __hip_atomic_fetch_add((LAS unsigned*)&ctl[w], 1u, __ATOMIC_RELAXED, __HIP_MEMORY_SCOPE_WORKGROUP); };
  auto nap_dry = [&]() { if (dry) { while (rt() - t_start < 250ull) __builtin_amdgcn_s_sleep(8); } };
  u64 npass = 0;
  auto gather = [&](auto& v, auto& off, unsigned tA, unsigned tB, auto nap, auto&& shadow) -> bool {
    constexpr int N = sizeof(v) / sizeof(v[0]);
#pragma unroll
    for (int j = 0; j < N; ++j) v[j] = v2u{0u, 0u};
    return spin([&] {
      if constexpr (STAMPS) npass += 100;
      v4u t[N];
#pragma unroll
      for (int j = 0; j < N; ++j) t[j] = ld(off[j]);
      __builtin_amdgcn_sched_barrier(0);
      shadow();
      __builtin_amdgcn_sched_barrier(0);
      bool pend = false;
#pragma unroll
      for (int j = 0; j < N; ++j) {
        const bool hit = (t[j][1] == tA) & (t[j][3] == tB) & (off[j] != OOB);
        v[j][0] = hit ? t[j][0] : v[j][0];
        v[j][1] = hit ? t[j][2] : v[j][1];
        off[j] = hit ? OOB : off[j];
        pend = pend | (off[j] != OOB);
      }
      return __any(pend) == 0;
    }, nap);
  };

  if (wave == NCONS + 1 || wave == NCONS + 3) {
    const u64 ep = __hip_atomic_load((GAS u64*)(p.ws + WS_EPOCH + 256 * br), __ATOMIC_RELAXED, __HIP_MEMORY_SCOPE_AGENT);
    __builtin_amdgcn_s_barrier();
    const unsigned tA = rfl((unsigned)ep) + 1u, tB = rfl((unsigned)(ep >> 32)) + 1u;
    put(W_TAGA, tA); put(W_TAGB, tB);
    LDS_WAIT();
    lds_add(W_QCNT);
    if (!spin([&] { return rfl(ctl[W_QPUB]) != 0u; }, Nap<1>{})) return;
    if constexpr (QDELAY > 0) { const u64 tq0 = rt(); while (rt() - tq0 < (u64)QDELAY) __builtin_amdgcn_s_sleep(2); }
    {
      v2u qv[16];
      int off[16];
#pragma unroll
      for (int j = 0; j < 16; ++j) off[j] = WS_Q + (grp * 2 + b) * 16384 + lane * 16 + 1024 * j;
      if (!gather(qv, off, tA, tB, Nap<4>{}, [] {})) return;
#pragma unroll
      for (int j = 0; j < 8; ++j)
        *(LAS unsigned*)(L + L_QIMG + j * 256 + lane * 4) = cvtpk(f(qv[j][0]) + f(qv[8 + j][0]), f(qv[j][1]) + f(qv[8 + j][1]));
      LDS_WAIT();
      put(W_QRDY, 1u);
    }
    stamp(0);
    sv[3] = npass; npass = 0;
    if constexpr (BAL) {
      if (!spin([&] { return rfl(ctl[W_TOK]) != 0u; }, Nap<1>{})) return;
      const int roff = WS_RATE + (bid >> 4) * 256;
      st(v4u{rfl(ctl[W_T1]), tA, rfl(ctl[W_T2]), tB}, lane == 0 ? roff + split * 16 : OOB);
      v2u rv[1];
      int off[1] = {roff + c * 16};
      if (!gather(rv, off, tA, tB, Nap<1>{}, [] {})) return;
      const float dt = fmaxf((float)(int)(rv[0][1] - rv[0][0]), 1.0f);
      const float rate = (float)(P2 - P1) / dt;
      const float tau = (float)(int)(rv[0][1] - rfl(rv[0][1]));
      float sr = rate, srt = rate * tau;
      sr += dpp<ROR1>(sr); srt += dpp<ROR1>(srt); sr += dpp<ROR2>(sr); srt += dpp<ROR2>(srt);
      sr += dpp<ROR4>(sr); srt += dpp<ROR4>(srt); sr += dpp<ROR8>(sr); srt += dpp<ROR8>(srt);
      sr = f(rfl(u(sr))); srt = f(rfl(u(srt)));
      const float T = ((float)(POOL + 16 * (NS - P2)) + srt) / sr;
      float n = fmaxf(rate * (T - tau) - (float)(NS - P2), 0.0f);
      n += dpp<SHR1>(n); n += dpp<SHR2>(n); n += dpp<SHR4>(n); n += dpp<SHR8>(n);
      const float tot = f((unsigned)__builtin_amdgcn_readlane((int)u(n), 15));
      unsigned bound = (unsigned)fminf(fmaxf(rintf((float)POOL * (n / tot)), 0.0f), (float)POOL);
      bound = c == 15 ? (unsigned)POOL : bound;
      const unsigned hi = (unsigned)__builtin_amdgcn_readlane((int)bound, split);
      const unsigned lo = split == 0 ? 0u : (unsigned)__builtin_amdgcn_readlane((int)bound, (split + 15) & 15);
      put(W_END, (unsigned)(KV0 + NS) + (hi - lo));
      put(W_DEAL, 0x80000000u | ((hi - lo) << 12) | lo);
    }
    if (!spin([&] { return rfl(ctl[W_ORDY]) >= (unsigned)NCONS; }, Nap<1>{})) return;
    stamp(1);
    v2u r[4];
    int off[4];
#pragma unroll
    for (int j = 0; j < 4; ++j) off[j] = WS_H2 + bid * 4096 + lane * 16 + 1024 * j;
    if (!gather(r, off, tA, tB, Nap<0>{}, [] {})) return;
    float s0 = (f(r[0][0]) + f(r[1][0])) + (f(r[2][0]) + f(r[3][0])), s1 = (f(r[0][1]) + f(r[1][1])) + (f(r[2][1]) + f(r[3][1]));
    s0 += dpp<ROR8>(s0); s1 += dpp<ROR8>(s1);
    s0 = xrow_sum(s0); s1 = xrow_sum(s1);
    if (lane < 8 && !dry) {
      const int oc = bid & 127;
      const int col = 64 * (oc >> 3) + 16 * (lane >> 1) + 4 * ((oc >> 1) & 3) + 2 * (oc & 1);
      *(GAS unsigned*)(p.out + (lane & 1) * 2048 + br * 1024 + col) = cvtpk(s0, s1);
    }
    if ((bid & 127) == 0 && lane == 0 && !dry)
      __hip_atomic_store((GAS u64*)(p.ws + WS_EPOCH + 256 * br), ((u64)tB << 32) | tA, __ATOMIC_RELAXED, __HIP_MEMORY_SCOPE_AGENT);
    stamp(2);
    sv[4] = npass;
    flush(16, 5);
    return;
  }

  const int ci = dry ? 0 : wave - 1;
  const int tq = (lane >> 2) & 3, tp = lane & 3;
  const v4u zero4 = {0u, 0u, 0u, 0u};

  v4u xf[4];
#pragma unroll
  for (int ks = 0; ks < 4; ++ks) xf[ks] = *(const GAS v4u*)(p.x[br] + (c & 1) * 1024 + b * 512 + ci * 128 + ks * 32 + 8 * g);
  __builtin_amdgcn_s_barrier();
  vmwait<0>();
  {
    const LAS unsigned char* s = L + L_RING + ci * SLOT + (8 * g + tq) * 128 + tp * 8;
    v4u w0[8], w1[8];
#pragma unroll
    for (int j = 0; j < 8; ++j) {
      const int o = (j >> 2) * 4096 + (j & 3) * 32;
      w0[j] = trfrag(s + o, s + o + 512); w1[j] = trfrag(s + 8192 + o, s + 8192 + o + 512);
    }
    LDS_WAIT();
    put(W_FREE + ci, (unsigned)(ci + 1));
    PIN8(w0); PIN8(w1);
    f32x4 a0 = {0.f, 0.f, 0.f, 0.f}, a1 = {0.f, 0.f, 0.f, 0.f};
#pragma unroll
    for (int j = 0; j < 8; ++j) {
      a0 = mfma(w0[j], (c >> 1) == (j & 3) ? xf[j >> 2] : zero4, a0);
      a1 = mfma(w1[j], (c >> 1) == (j & 3) ? xf[2 + (j >> 2)] : zero4, a1);
    }
    if (c < 8) *(LAS f32x4*)(L + L_QRED + ci * 512 + (c & 1) * 256 + (c >> 1) * 64 + g * 16) = a0 + a1;
    LDS_WAIT();
    lds_add(W_QCNT);
  }
  stamp(2);
  if (!spin([&] { return rfl(ctl[W_QCNT]) >= (unsigned)(NCONS + 1); }, Nap<0>{})) return;
  __builtin_amdgcn_fence(__ATOMIC_ACQUIRE, "workgroup");
  const unsigned tA = rfl(ctl[W_TAGA]), tB = rfl(ctl[W_TAGB]);
  if (ci == 0) {
    float a0 = 0.f, a1 = 0.f;
#pragma unroll
    for (int w = 0; w < NCONS; ++w) { const LAS float* s = (const LAS float*)(L + L_QRED + w * 512 + lane * 8); a0 += s[0]; a1 += s[1]; }
    st(v4u{u(a0), tA, u(a1), tB}, WS_Q + ((grp * 2 + (lane >> 5)) * 2 + b) * 8192 + split * 512 + (lane & 31) * 16);
    put(W_QPUB, 1u);
  }
  stamp(3);
  v4u wf[4][4];
  auto wo_frags = [&](unsigned fl, int ks) {
    const LAS unsigned char* s = L + L_RING + (fl & (NSLOT - 1)) * SLOT + (8 * g + tq) * 512 + ci * 128 + tp * 8;
#pragma unroll
    for (int e = 0; e < 4; ++e) wf[ks][e] = trfrag(s + e * 32, s + e * 32 + 2048);
  };
  if constexpr (WOE > 0) {
    if (!spin([&] { return rfl(ctl[W_FULL + ((KV0 - 1) & (NSLOT - 1))]) >= (unsigned)KV0; }, Nap<1>{})) return;
    __builtin_amdgcn_fence(__ATOMIC_ACQUIRE, "workgroup");
#pragma unroll
    for (int ks = 0; ks < WOE; ++ks) wo_frags(4 + ks, ks);
    LDS_WAIT();
#pragma unroll
    for (int ks = 0; ks < WOE; ++ks) asm volatile("" : "+v"(wf[ks][0]), "+v"(wf[ks][1]), "+v"(wf[ks][2]), "+v"(wf[ks][3]) : : "memory");
    unsigned old = 0u;
    if (lane == 0) old = __hip_atomic_fetch_add((LAS unsigned*)&ctl[W_WCNT], 1u, __ATOMIC_RELAXED, __HIP_MEMORY_SCOPE_WORKGROUP);
    if (rfl(old) == NCONS - 1) {
#pragma unroll
      for (int ks = 0; ks < WOE; ++ks) ctl[W_FREE + 4 + ks] = (unsigned)(4 + ks + 1);
    }
  }

  v4u qf[4] = {zero4, zero4, zero4, zero4};
  f32x4 acc[8];
#pragma unroll
  for (int e = 0; e < 8; ++e) acc[e] = f32x4{0.f, 0.f, 0.f, 0.f};
  float m = -INFINITY, l = 0.f;
  unsigned kra[4], vra[8];
#pragma unroll
  for (int s = 0; s < 4; ++s) kra[s] = (unsigned)c * 256u + (unsigned)(((4 * s + g) ^ c) * 16);
#pragma unroll
  for (int e = 0; e < 8; ++e) vra[e] = 8192u + (unsigned)(4 * g + tq) * 256u + (unsigned)((e ^ (4 * (g & 1) + tq)) * 32 + tp * 8);
  int fl = KV0 + ci;
  u64 tx[4] = {};
#pragma unroll 1
  for (;; fl += NCONS) {
    bool done = false;
    if (!spin([&] {
          const unsigned fu = rfl(ctl[W_FULL + (fl & (NSLOT - 1))]);
          const unsigned en = BAL ? rfl(ctl[W_END]) : (unsigned)(KV0 + NS);
          done = (unsigned)fl >= en;
          return done || fu >= (unsigned)(fl + 1);
        }, Nap<1>{})) return;
    if (done || (dry && fl >= KV0 + NCONS)) break;
    if constexpr (STAMPS) tx[0] = rt();
    __builtin_amdgcn_fence(__ATOMIC_ACQUIRE, "workgroup");
    const LAS unsigned char* s = L + L_RING + (fl & (NSLOT - 1)) * SLOT;
    v4u K[8], V[8];
#pragma unroll
    for (int j = 0; j < 8; ++j) K[j] = *(const LAS v4u*)(s + kra[j & 3] + (j >> 2) * 4096);
#pragma unroll
    for (int e = 0; e < 8; ++e) V[e] = trfrag(s + vra[e], s + vra[e] + 4096);
    LDS_WAIT();
    put(W_FREE + (fl & (NSLOT - 1)), (unsigned)(fl + 1));
    PIN8(K); PIN8(V);
    if constexpr (STAMPS) tx[1] = rt();
    if (fl < KV0 + NCONS) {
      if (!spin([&] { return rfl(ctl[W_QRDY]) != 0u; }, Nap<1>{})) return;
      __builtin_amdgcn_fence(__ATOMIC_ACQUIRE, "workgroup");
#pragma unroll
      for (int j = 0; j < 4; ++j) {
        const v4u t = *(const LAS v4u*)(L + L_QIMG + (c & 7) * 256 + j * 64 + g * 16);
        qf[j] = c < 8 ? t : zero4;
      }
      stamp(4);
    }
    f32x4 s0 = {0.f, 0.f, 0.f, 0.f}, s1 = {0.f, 0.f, 0.f, 0.f};
#pragma unroll
    for (int j = 0; j < 4; ++j) { s0 = mfma(K[j], qf[j], s0); s1 = mfma(K[4 + j], qf[j], s1); }
    float tm = fmaxf(fmaxf(fmaxf(s0[0], s0[1]), fmaxf(s0[2], s0[3])), fmaxf(fmaxf(s1[0], s1[1]), fmaxf(s1[2], s1[3])));
    tm = xrow_max(tm) * C2;
    if (__any(tm > m)) {
      const float mn = fmaxf(m, tm);
      const float alpha = __builtin_amdgcn_exp2f(m - mn);
      m = mn;
      l *= alpha;
#pragma unroll
      for (int e = 0; e < 8; ++e) acc[e] *= alpha;
    }
    float pr[8];
#pragma unroll
    for (int i = 0; i < 4; ++i) { pr[i] = __builtin_amdgcn_exp2f(fmaf(s0[i], C2, -m)); pr[4 + i] = __builtin_amdgcn_exp2f(fmaf(s1[i], C2, -m)); }
    const v4u pf = {cvtpk(pr[0], pr[1]), cvtpk(pr[2], pr[3]), cvtpk(pr[4], pr[5]), cvtpk(pr[6], pr[7])};
#pragma unroll
    for (int e = 0; e < 8; ++e) acc[e] = mfma(V[e], pf, acc[e]);
    l += ((pr[0] + pr[1]) + (pr[2] + pr[3])) + ((pr[4] + pr[5]) + (pr[6] + pr[7]));
  }

  stamp(5);
  l = xrow_sum(l);
  if (c < 8 && !dry) {
    LAS unsigned char* row = L + L_MRG + (ci * 8 + c) * MROW + g * 16;
#pragma unroll
    for (int e = 0; e < 8; ++e) *(LAS f32x4*)(row + e * 64) = acc[e];
    if (g == 0) { LAS float* mm = (LAS float*)(L + L_MML) + (ci * 8 + c) * 2; mm[0] = m; mm[1] = l; }
  }
  LDS_WAIT();
  lds_add(W_MCNT);
  if constexpr (STAMPS) tx[2] = rt();
  if (!spin([&] { return rfl(ctl[W_MCNT]) >= (unsigned)NCONS; }, Nap<0>{})) return;
  if constexpr (STAMPS) tx[3] = rt();
  __builtin_amdgcn_fence(__ATOMIC_ACQUIRE, "workgroup");
  {
    const int h = 2 * ci + (lane >> 5), l5 = lane & 31;
    float mw[NCONS], lw[NCONS];
    f32x4 aw[NCONS];
#pragma unroll
    for (int w = 0; w < NCONS; ++w) {
      const LAS float* mm = (const LAS float*)(L + L_MML) + (w * 8 + h) * 2;
      mw[w] = mm[0]; lw[w] = mm[1];
      aw[w] = *(const LAS f32x4*)(L + L_MRG + (w * 8 + h) * MROW + l5 * 16);
    }
    const float M = fmaxf(fmaxf(mw[0], mw[1]), fmaxf(mw[2], mw[3]));
    float Ls = 0.f;
    f32x4 o = {0.f, 0.f, 0.f, 0.f};
#pragma unroll
    for (int w = 0; w < NCONS; ++w) { const float wt = __builtin_amdgcn_exp2f(mw[w] - M); Ls = fmaf(wt, lw[w], Ls); o += aw[w] * wt; }
    const int rec = WS_H1 + ((grp * 16 + h * 2 + (l5 >> 4)) * 32 + gi) * 512 + ((l5 >> 3) & 1) * 256 + (l5 & 7) * 16;
    st(v4u{u(o[0]), tA, u(o[1]), tB}, rec);
    st(v4u{u(o[2]), tA, u(o[3]), tB}, rec + 128);
    st(v4u{u(M), tA, u(Ls), tB}, l5 < (MLF ? 8 : 1) ? WS_ML + ((grp * 8 + h) * 32 + gi) * MLS + l5 * 16 : OOB);
  }
  stamp(6);

  const unsigned wo0 = (BAL ? rfl(ctl[W_END]) : (unsigned)(KV0 + NS)) - WOE;
  bool have_wo = WOE == 4;
  auto try_wo = [&]() -> bool {
    if (rfl(ctl[W_FULL + ((wo0 + 3) & (NSLOT - 1))]) < wo0 + 4u) return false;
    __builtin_amdgcn_fence(__ATOMIC_ACQUIRE, "workgroup");
#pragma unroll
    for (int ks = WOE; ks < 4; ++ks) wo_frags(wo0 + ks, ks);
    have_wo = true;
    return true;
  };
  {
    v2u d[9];
    int off[9];
    const int rec = WS_H1 + ((grp * 16 + (gi & 7) * 2 + (ci & 1)) * 32 + (ci >> 1) * 16) * 512 + lane * 16;
#pragma unroll
    for (int j = 0; j < 8; ++j) off[j] = rec + 1024 * j;
    off[8] = WS_ML + ((grp * 8 + (gi & 7)) * 32 + (ci >> 1) * 16 + (c ^ (lane >> 5))) * MLS;
    if (!gather(d, off, tA, tB, Nap<NAP1>{}, [&] { if (!have_wo) try_wo(); })) return;
    float M = f(d[8][0]);
    M = fmaxf(M, dpp<ROR1>(M)); M = fmaxf(M, dpp<ROR2>(M)); M = fmaxf(M, dpp<ROR4>(M)); M = fmaxf(M, dpp<ROR8>(M));
    const float w = __builtin_amdgcn_exp2f(f(d[8][0]) - M);
    float Ls = w * f(d[8][1]);
    Ls += dpp<ROR1>(Ls); Ls += dpp<ROR2>(Ls); Ls += dpp<ROR4>(Ls); Ls += dpp<ROR8>(Ls);
    float o0 = 0.f, o1 = 0.f;
#define ACC(J) { const float wj = dpp<0x150 + 2 * J>(w); o0 = fmaf(wj, f(d[J][0]), o0); o1 = fmaf(wj, f(d[J][1]), o1); }
    ACC(0) ACC(1) ACC(2) ACC(3) ACC(4) ACC(5) ACC(6) ACC(7)
#undef ACC
    const auto x0 = __builtin_amdgcn_permlane32_swap(u(o0), u(o0), false, false);
    const auto x1 = __builtin_amdgcn_permlane32_swap(u(o1), u(o1), false, false);
    const float inv = 1.0f / Ls;
    const unsigned pk = cvtpk((f(x0[0]) + f(x0[1])) * inv, (f(x1[0]) + f(x1[1])) * inv);
    if (lane < 32) *(LAS unsigned*)(L + L_OFRAG + ci * 128 + (lane >> 4) * 64 + (lane & 7) * 8 + ((lane >> 3) & 1) * 4) = pk;
    LDS_WAIT();
    lds_add(W_ORDY);
  }
  stamp(1);

  if (!have_wo && !spin(try_wo, Nap<0>{})) return;
  if (!spin([&] { return rfl(ctl[W_ORDY]) >= (unsigned)NCONS; }, Nap<0>{})) return;
  __builtin_amdgcn_fence(__ATOMIC_ACQUIRE, "workgroup");
  f32x4 a0 = {0.f, 0.f, 0.f, 0.f}, a1 = {0.f, 0.f, 0.f, 0.f};
#pragma unroll
  for (int ks = 0; ks < 4; ++ks) {
    const v4u of = *(const LAS v4u*)(L + L_OFRAG + (c & 1) * 256 + ks * 64 + g * 16);
#pragma unroll
    for (int e = 0; e < 4; e += 2) {
      a0 = mfma(wf[ks][e], (c >> 1) == e ? of : zero4, a0);
      a1 = mfma(wf[ks][e + 1], (c >> 1) == e + 1 ? of : zero4, a1);
    }
  }
  a0 += a1;
  {
    const int o0 = c < 8 ? WS_H2 + (br * 128 + (((gi >> 3) * 4 + ci) * 4 + g) * 2) * 4096 + (kvh * 8 + (gi & 7)) * 128 + c * 16 : OOB - 4096;
    st(v4u{u(a0[0]), tA, u(a0[1]), tB}, o0);
    st(v4u{u(a0[2]), tA, u(a0[3]), tB}, o0 + 4096);
  }
  stamp(7);
  sv[0] = t_start;
  if (wave == 1) flush(0, 8);
  if (wave == 1) { sv[0] = npass; flush(21, 1); }
  if constexpr (STAMPS) { if (wave == 4 && lane == 0) { u64* s2 = stamps + 8192; s2[0] = tx[0]; s2[1] = tx[1]; s2[2] = sv[5]; s2[3] = tx[2]; s2[4] = tx[3]; s2[5] = sv[6]; s2[6] = sv[1]; s2[7] = sv[7]; } }
}

template <int WIN, int MLF, int QDELAY, int WOE, int STAMPS>
static void launch(void* const* d_in, void* d_out, void* d_ws, hipStream_t stream) {
  Params p;
  for (int br = 0; br < 2; ++br) {
    p.x[br] = (const u16*)d_in[5 * br + 0]; p.k[br] = (const u16*)d_in[5 * br + 1]; p.v[br] = (const u16*)d_in[5 * br + 2];
    p.wq[br] = (const u16*)d_in[5 * br + 3]; p.wo[br] = (const u16*)d_in[5 * br + 4];
  }
  p.out = (u16*)d_out; p.ws = (unsigned char*)d_ws;
  (void)hipFuncSetAttribute((const void*)k_fused<WIN, MLF, QDELAY, WOE, STAMPS>, hipFuncAttributeMaxDynamicSharedMemorySize, L_BYTES);
  hipLaunchKernelGGL((k_fused<WIN, MLF, QDELAY, WOE, STAMPS>), dim3(NWG), dim3(64 * (NCONS + 2)), L_BYTES, stream, p);
}

}

extern "C" void kernel_launch(void* const* d_in, const int* in_sizes, int n_in, void* d_out, int out_size, void* d_ws, size_t ws_size,
                              hipStream_t stream) {
  fused::launch<CFG>(d_in, d_out, d_ws, stream);
}
```

```cpp
#include <hip/hip_runtime.h>
#include <hip/hip_bf16.h>
#include <cstdint>

#define GAS __attribute__((address_space(1)))
#define LAS __attribute__((address_space(3)))
typedef unsigned v4u __attribute__((ext_vector_type(4)));
typedef unsigned v2u __attribute__((ext_vector_type(2)));
typedef float f32x4 __attribute__((ext_vector_type(4)));
typedef float f32x2 __attribute__((ext_vector_type(2)));
typedef short bf16x8 __attribute__((ext_vector_type(8)));
typedef short s16x4 __attribute__((ext_vector_type(4)));
typedef unsigned short u16;
typedef unsigned long long u64;

#define CFG 16, 15, 30, 250, 0

namespace fused {

constexpr int SK = 32768, DH = 128;
constexpr int NWG = 256, NCONS = 4, NWAVE = 10;
constexpr int NSLOT = 16, SLOT = 8192;
constexpr int NT = 32, TPC = NT / NCONS;
constexpr int F_KA = 8, F_VA = F_KA + NT, F_WOA = F_VA + NT, F_KB = F_WOA + 4, F_VB = F_KB + NT, F_WOB = F_VB + NT;
constexpr float C2 = 0.088388347648318440f * 1.4426950408889634f;
constexpr u64 TCAP = 2000000;
constexpr int OOB = 0x7ffffff0;

constexpr int WS_EPOCH = 0;
constexpr int WS_ML = 4096;
constexpr int WS_Q = WS_ML + 2 * 8 * 4096;
constexpr int WS_H2 = WS_Q + 2 * 8 * 16384;
constexpr int WS_H1 = WS_H2 + 256 * 4096;
constexpr int WS_END = WS_H1 + 2 * 128 * 8192;
constexpr int WS_STAMP = WS_END;

constexpr int L_RING = 0;
constexpr int L_CTL = NSLOT * SLOT;
constexpr int L_QIMG = L_CTL + 512;
constexpr int L_QRED = L_QIMG + 4096;
constexpr int L_OIMG = L_QRED + 2048;
constexpr int L_STAT = L_OIMG + 1024;
constexpr int MROW = 528;
constexpr int L_MRG = L_STAT + 512;
constexpr int L_BYTES = L_MRG + 4 * 8 * MROW;
enum { W_FULL = 0, W_FREE = 16, W_ABORT = 32, W_TAGA, W_TAGB, W_TAGOK, W_WIDE, W_STAMP,
       W_QCNT = 40, W_QPUB, W_QRDY, W_XCNT, W_LCNT, W_SRDY, W_MCNT, W_ORDY, W_WCNT, W_PER = 16 };
static_assert(L_BYTES <= 160 * 1024 && L_BYTES > 80 * 1024, "one workgroup per CU");

struct Params { const u16* x[2]; const u16* k[2]; const u16* v[2]; const u16* wq[2]; const u16* wo[2]; u16* out; unsigned char* ws; };

__device__ __forceinline__ u64 rt() { return __builtin_amdgcn_s_memrealtime(); }
__device__ __forceinline__ unsigned rfl(unsigned v) { return (unsigned)__builtin_amdgcn_readfirstlane((int)v); }
__device__ __forceinline__ unsigned cvtpk(float lo, float hi) {
  unsigned r; asm("v_cvt_pk_bf16_f32 %0, %1, %2" : "=v"(r) : "v"(lo), "v"(hi)); return r;
}
__device__ __forceinline__ f32x4 mfma(v4u a, v4u b, f32x4 c) {
  return __builtin_amdgcn_mfma_f32_16x16x32_bf16(__builtin_bit_cast(bf16x8, a), __builtin_bit_cast(bf16x8, b), c, 0, 0, 0);
}
__device__ __forceinline__ float f(unsigned u) { return __uint_as_float(u); }
__device__ __forceinline__ unsigned u(float x) { return __float_as_uint(x); }
__device__ __forceinline__ float xrow_max(float x) {
  auto s = __builtin_amdgcn_permlane16_swap(u(x), u(x), false, false);
  x = fmaxf(f(s[0]), f(s[1]));
  auto t = __builtin_amdgcn_permlane32_swap(u(x), u(x), false, false);
  return fmaxf(f(t[0]), f(t[1]));
}
__device__ __forceinline__ float xrow_sum(float x) {
  auto s = __builtin_amdgcn_permlane16_swap(u(x), u(x), false, false);
  x = f(s[0]) + f(s[1]);
  auto t = __builtin_amdgcn_permlane32_swap(u(x), u(x), false, false);
  return f(t[0]) + f(t[1]);
}
template <int CTRL> __device__ __forceinline__ float dpp(float x) {
  return __builtin_bit_cast(float, __builtin_amdgcn_mov_dpp(__builtin_bit_cast(int, x), CTRL, 0xf, 0xf, true));
}
constexpr int ROR1 = 0x121, ROR2 = 0x122, ROR4 = 0x124, ROR8 = 0x128, BCAST = 0x150;
template <int N> struct IC { static constexpr int value = N; };
template <int I, int E, class F> __device__ __forceinline__ void each(F& fn) {
  if constexpr (I < E) { fn(IC<I>{}); each<I + 1, E>(fn); }
}
template <int N> __device__ __forceinline__ void vmwait() { asm volatile("s_waitcnt vmcnt(%0)" ::"i"(N) : "memory"); }
#define LDS_WAIT() asm volatile("s_waitcnt lgkmcnt(0)" ::: "memory")
#define LDS_ACQ() __builtin_amdgcn_fence(__ATOMIC_ACQUIRE, "workgroup")
#define SCHED() __builtin_amdgcn_sched_barrier(0)
#define PIN8(w) asm volatile("" : "+v"(w[0]), "+v"(w[1]), "+v"(w[2]), "+v"(w[3]), "+v"(w[4]), "+v"(w[5]), "+v"(w[6]), "+v"(w[7]) : : "memory")
#define GLDS(src, dst, imm) __builtin_amdgcn_global_load_lds((const GAS unsigned*)(src), (LAS unsigned*)(dst), 16, (imm), 2)

__device__ __forceinline__ v4u trfrag(const LAS unsigned char* lo, const LAS unsigned char* hi) {
  const v2u a = __builtin_bit_cast(v2u, __builtin_amdgcn_ds_read_tr16_b64_v4i16((LAS s16x4*)lo));
  const v2u b = __builtin_bit_cast(v2u, __builtin_amdgcn_ds_read_tr16_b64_v4i16((LAS s16x4*)hi));
  return v4u{a[0], a[1], b[0], b[1]};
}

template <int WX, int GAIN, int DEAD, int DH1, int STAMPS>
__global__ void __launch_bounds__(64 * NWAVE) k_fused(Params p) {
  constexpr int DQB = 600, QDELAY = 100, RT = 5, STAG = 0, WN = 16, HALF = 0, ALATE = 0;
  static_assert(WN % 4 == 0 && (HALF || WN % 8 == 0) && WN >= 8 && WN <= 48 && RT >= 0 && RT <= 5, "pieces left in flight; run of 2^RT tiles");
  extern __shared__ __attribute__((aligned(16))) unsigned char lds_raw[];
  LAS unsigned char* L = (LAS unsigned char*)lds_raw;
  volatile LAS unsigned* ctl = (volatile LAS unsigned*)(L + L_CTL);
  const u64 t_start = rt();
  const int bid = blockIdx.x, sidx = bid >> 5, part = bid & 31, b = sidx >> 2, kvh = sidx & 3;
  const int tid = threadIdx.x, lane = tid & 63, wave = (int)rfl(tid >> 6);
  const int c = lane & 15, g = lane >> 4;
  if (__builtin_expect(wave >= 1 && wave <= NCONS, 1)) {
    const unsigned char* wq = (const unsigned char*)p.wq[bid >> 7] + (size_t)(((bid >> 6) & 1) * 512 + (wave - 1) * 128) * 8192 + (bid & 63) * 128;
    const unsigned wq_off = (unsigned)(lane >> 3) * 8192u + (unsigned)(lane & 7) * 16u;
#pragma unroll
    for (int j = 0; j < 16; ++j) GLDS(wq + (size_t)j * 8 * 8192 + wq_off, L + L_RING + (wave - 1) * 2 * SLOT + j * 1024, 0);
  }
  u64* stamps = (u64*)(p.ws + WS_STAMP) + bid * 32;
  auto stamp = [&](int i) { if constexpr (STAMPS) { const u64 t = rt(); if (lane == 0) *(volatile LAS u64*)(L + L_BYTES + i * 8) = t; } };

  auto spin = [&](auto&& cond, auto nap) {
    for (unsigned it = 1; !cond(); ++it) {
      if constexpr (decltype(nap)::value > 0) __builtin_amdgcn_s_sleep(decltype(nap)::value);
      if ((it & 63u) == 0u) {
        if (rt() - t_start > TCAP) ctl[W_ABORT] = 1u;
        if (rfl(ctl[W_ABORT])) break;
      }
    }
  };
  auto wait_ge = [&](int w, unsigned n, auto nap) {
    spin([&] { return rfl(ctl[w]) >= n; }, nap);
    LDS_ACQ();
  };

  if (__builtin_expect(wave == 0, 1)) {
    __builtin_amdgcn_s_setprio(3);
    ctl[lane] = 0u; ctl[64 + lane] = 0u;
    unsigned koff[4], voff[2];
#pragma unroll
    for (int j = 0; j < 4; ++j) koff[j] = (unsigned)g * 256u + (unsigned)((c ^ (4 * j + g)) * 16);
#pragma unroll
    for (int j = 0; j < 2; ++j) voff[j] = (unsigned)g * 256u + (unsigned)((c ^ (2 * (4 * j + g))) * 16);
    constexpr int LAG = WN / 8 + 1;
    int fill = F_KA, lastpark = 0;
    u64 parked = 0;
    unsigned fr = 0u;
    auto full = [&](int fl) { ctl[W_FULL + (fl & (NSLOT - 1))] = (unsigned)(fl + 1); };
    auto drain = [&]() {
      vmwait<24>(); full(fill - 4); vmwait<16>(); full(fill - 3); vmwait<8>(); full(fill - 2); vmwait<0>(); full(fill - 1);
#pragma unroll
      for (int j = 5; j <= LAG + WX / 8; ++j) full(fill - j);
    };
    auto room = [&]() {
      if (rfl(fr) < (unsigned)(fill - NSLOT + 1) && fill >= NSLOT) {
        const u64 tp = rt();
        drain();
        spin([&] { return rfl(ctl[W_FREE + (fill & (NSLOT - 1))]) >= (unsigned)(fill - NSLOT + 1); }, IC<1>{});
        parked += rt() - tp; lastpark = fill;
      }
    };
    unsigned wd = 0u;
    int until = -1;
    auto issue = [&](const unsigned char* s0, const unsigned char* s1, unsigned o0, unsigned o1, unsigned o2, unsigned o3) {
      const unsigned dst = (unsigned)(size_t)(L + L_RING) + (unsigned)((fill & (NSLOT - 1)) * SLOT);
      room();
      if (WX > 0 && until < 0 && rfl(wd) != 0u) until = fill + (int)rfl(wd) - 1;
#define FILL(N) asm volatile("s_mov_b32 m0, %0\n\ts_waitcnt vmcnt(%7)\n\t"                                                     \
                   "global_load_lds_dwordx4 %3, %1 nt\n\tglobal_load_lds_dwordx4 %4, %1 offset:1024 nt\n\t"                    \
                   "global_load_lds_dwordx4 %5, %1 offset:2048 nt\n\tglobal_load_lds_dwordx4 %6, %1 offset:3072 nt\n\t"        \
                   "s_add_u32 m0, %0, 0x1000\n\ts_nop 0\n\t"                                                                  \
                   "global_load_lds_dwordx4 %3, %2 nt\n\tglobal_load_lds_dwordx4 %4, %2 offset:1024 nt\n\t"                    \
                   "global_load_lds_dwordx4 %5, %2 offset:2048 nt\n\tglobal_load_lds_dwordx4 %6, %2 offset:3072 nt"            \
                   :: "s"(dst), "s"(s0), "s"(s1), "v"(o0), "v"(o1), "v"(o2), "v"(o3), "i"(N) : "memory", "scc")
      if (WX > 0 && fill < until) {
        FILL(WN + WX);
        full(fill - LAG - WX / 8);
      } else {
        FILL(WN);
#pragma unroll
        for (int j = WX / 8; j >= 0; --j) full(fill - LAG - j);
      }
#undef FILL
      ++fill;
      fr = ctl[W_FREE + (fill & (NSLOT - 1))];
      if (WX > 0) wd = ctl[W_WIDE];
    };
    const size_t sbase = (size_t)sidx * SK * DH * 2;
    auto tile_off = [&](int i) -> size_t {
      constexpr int NR = NT >> RT;
      return (size_t)((((((i >> RT) + STAG * sidx) & (NR - 1)) * 32 + part) << RT) | (i & ((1 << RT) - 1))) * 8192;
    };
    const size_t wo_base = (size_t)((bid & 31) * 128) * 2048 + (bid >> 5) * 256;
    const unsigned woff = (unsigned)g * 2048u + (unsigned)c * 16u;
    LDS_WAIT();
    __builtin_amdgcn_s_barrier();
    stamp(11);
#pragma unroll 1
    for (int br = 0; br < 2; ++br) {
      const unsigned char* kb = (const unsigned char*)p.k[br] + sbase;
      const unsigned char* vb = (const unsigned char*)p.v[br] + sbase;
#pragma unroll 1
      for (int i = 0; i < NT; ++i) { const unsigned char* s = kb + tile_off(i); issue(s, s + 4096, koff[0], koff[1], koff[2], koff[3]); }
      stamp(24 + 2 * br);
#pragma unroll 1
      for (int i = 0; i < NT; ++i) { const unsigned char* s = vb + tile_off(i); issue(s, s + 4096, voff[0], voff[1], voff[0], voff[1]); }
      stamp(25 + 2 * br);
#pragma unroll 1
      for (int i = 0; i < 4; ++i) {
        const unsigned char* s = (const unsigned char*)p.wo[br] + wo_base + (size_t)(i * 32) * 2048;
        issue(s, s + 4 * 8192, woff, woff + 7168u, woff + 14336u, woff + 21504u);
      }
    }
    drain();
    stamp(9);
    if constexpr (STAMPS) { if (lane == 0) { *(volatile LAS u64*)(L + L_BYTES + 20 * 8) = parked; *(volatile LAS u64*)(L + L_BYTES + 21 * 8) = (u64)lastpark * 100; } }
    return;
  }

  const __amdgpu_buffer_rsrc_t rs = __builtin_amdgcn_make_buffer_rsrc(p.ws, 0, WS_END, 0x00020000);
  auto ld = [&](int off) -> v4u { return __builtin_amdgcn_raw_buffer_load_b128(rs, off, 0, 16); };
  auto st = [&](v4u v, int off) { __builtin_amdgcn_raw_buffer_store_b128(v, rs, off, 0, 16); };
  auto lds_add = [&](int w) { if (lane == 0) __hip_atomic_fetch_add((LAS unsigned*)&ctl[w], 1u, __ATOMIC_RELAXED, __HIP_MEMORY_SCOPE_WORKGROUP); };
  auto delay = [&](u64 ticks) { const u64 t0 = rt(); while (rt() - t0 < ticks) __builtin_amdgcn_s_sleep(2); };
  auto poll = [&](auto& v, auto& off, unsigned tA, unsigned tB, auto nap, auto&& shadow) {
    constexpr int N = sizeof(v) / sizeof(v[0]);
    spin([&] {
      v4u t[N];
#pragma unroll
      for (int j = 0; j < N; ++j) t[j] = ld(off[j]);
      SCHED();
      shadow();
      SCHED();
      bool pend = false;
#pragma unroll
      for (int j = 0; j < N; ++j) {
        const bool hit = (t[j][1] == tA) & (t[j][3] == tB) & (off[j] != OOB);
        v[j][0] = hit ? t[j][0] : v[j][0];
        v[j][1] = hit ? t[j][2] : v[j][1];
        off[j] = hit ? OOB : off[j];
        pend = pend | (off[j] != OOB);
      }
      return __any(pend) == 0;
    }, nap);
  };
  auto gather = [&](auto& v, int base, unsigned tA, unsigned tB, auto nap, auto&& shadow) {
    constexpr int N = sizeof(v) / sizeof(v[0]);
    int off[N];
#pragma unroll
    for (int j = 0; j < N; ++j) { off[j] = base + lane * 16 + 1024 * j; v[j] = v2u{0u, 0u}; }
    poll(v, off, tA, tB, nap, shadow);
  };
  const int tq = (lane >> 2) & 3, tp = lane & 3;
  const v4u zero4 = {0u, 0u, 0u, 0u};
  const f32x4 zf = {0.f, 0.f, 0.f, 0.f};
  auto pick = [&](f32x4 a0, f32x4 a1, f32x4 a2, f32x4 a3) -> f32x4 {
    const f32x4 lo = (c & 2) ? a1 : a0, hi = (c & 2) ? a3 : a2;
    return (c & 4) ? hi : lo;
  };
  LAS float* smax = (LAS float*)(L + L_STAT);
  LAS float* ssum = smax + 32;

  if (wave == NCONS + 1) {
    const u64 ep = __hip_atomic_load((GAS u64*)(p.ws + WS_EPOCH), __ATOMIC_RELAXED, __HIP_MEMORY_SCOPE_AGENT);
    __builtin_amdgcn_s_barrier();
    const unsigned tA = rfl((unsigned)ep) + 1u, tB = rfl((unsigned)(ep >> 32)) + 1u;
    ctl[W_TAGA] = tA; ctl[W_TAGB] = tB;
    LDS_WAIT();
    ctl[W_TAGOK] = 1u;
    auto reduce = [&](auto nap) {
      v2u r[4];
      gather(r, WS_H2 + ((bid & 1) * 128 + (bid >> 1)) * 4096, tA, tB, nap, [] {});
      float s0 = (f(r[0][0]) + f(r[1][0])) + (f(r[2][0]) + f(r[3][0])), s1 = (f(r[0][1]) + f(r[1][1])) + (f(r[2][1]) + f(r[3][1]));
      s0 += dpp<ROR8>(s0); s1 += dpp<ROR8>(s1);
      s0 = xrow_sum(s0); s1 = xrow_sum(s1);
      if (lane < 8) {
        const int col = 32 * (bid >> 3) + 16 * ((lane >> 1) & 1) + 4 * ((bid >> 1) & 3) + 2 * (lane >> 2);
        *(GAS unsigned*)(p.out + (lane & 1) * 2048 + (bid & 1) * 1024 + col) = cvtpk(s0, s1);
      }
    };
    wait_ge(W_ORDY + W_PER, (unsigned)NCONS, IC<2>{});
    stamp(17);
    reduce(IC<0>{});
    if (bid == 1 && lane == 0)
      __hip_atomic_store((GAS u64*)(p.ws + WS_EPOCH), ((u64)tB << 32) | tA, __ATOMIC_RELAXED, __HIP_MEMORY_SCOPE_AGENT);
    if constexpr (STAMPS) {
      stamp(18);
      delay(300);
      if (lane < 32) stamps[lane] = lane == 0 ? t_start : *(volatile LAS u64*)(L + L_BYTES + lane * 8);
    }
    return;
  }

  if (wave > NCONS + 1) {
    const int tw = wave - NCONS - 2;
    __builtin_amdgcn_s_barrier();
    wait_ge(W_QPUB, 1u, IC<2>{});
    const unsigned tA = rfl(ctl[W_TAGA]), tB = rfl(ctl[W_TAGB]);
#pragma unroll 1
    for (int br = 0; br < 2; ++br) {
      delay(br ? DQB : QDELAY);
      v2u q[4];
      int off[4];
      const int qb = WS_Q + ((br * 4 + kvh) * 2 + b) * 16384 + tw * 2048 + lane * 16;
#pragma unroll
      for (int j = 0; j < 4; ++j) { off[j] = qb + (j >> 1) * 8192 + (j & 1) * 1024; q[j] = v2u{0u, 0u}; }
      poll(q, off, tA, tB, IC<4>{}, [] {});
      LAS unsigned* qi = (LAS unsigned*)(L + L_QIMG + br * 2048 + tw * 512 + lane * 4);
      qi[0] = cvtpk(f(q[0][0]) + f(q[2][0]), f(q[0][1]) + f(q[2][1]));
      qi[64] = cvtpk(f(q[1][0]) + f(q[3][0]), f(q[1][1]) + f(q[3][1]));
      LDS_WAIT();
      lds_add(W_QRDY + W_PER * br);
      if (tw == 0) stamp(15 + br);
    }
    v4u held = zero4;
#pragma unroll 1
    for (int br = 0; br < 2; ++br) {
      const int wb = W_PER * br, f0 = br ? F_WOB : F_WOA;
      const int o2 = c < 8 ? WS_H2 + (br * 128 + ((bid >> 5) * 4 + tw) * 4 + g) * 4096 + (bid & 31) * 128 + c * 16 : OOB;
      v4u wf[8];
      bool have_wo = false;
      auto try_wo = [&]() -> bool {
        if (rfl(ctl[W_FULL + ((f0 + 3) & (NSLOT - 1))]) < (unsigned)(f0 + 4)) return false;
        LDS_ACQ();
        const LAS unsigned char* s = L + L_RING + (f0 & (NSLOT - 1)) * SLOT + (8 * g + tq) * 256 + tw * 64 + tp * 8;
#pragma unroll
        for (int j = 0; j < 8; ++j) wf[j] = trfrag(s + (j >> 1) * 8192 + (j & 1) * 32, s + (j >> 1) * 8192 + (j & 1) * 32 + 1024);
        LDS_WAIT();
        PIN8(wf);
        unsigned old = 0u;
        if (lane == 0) old = __hip_atomic_fetch_add((LAS unsigned*)&ctl[W_WCNT + wb], 1u, __ATOMIC_RELAXED, __HIP_MEMORY_SCOPE_WORKGROUP);
        if (rfl(old) == 3u && lane < 4) ctl[W_FREE + ((f0 + lane) & (NSLOT - 1))] = (unsigned)(f0 + lane + 1);
        have_wo = true;
        return true;
      };
      v2u d[9];
      int off[9];
#pragma unroll
      for (int j = 0; j < 8; ++j) { off[j] = WS_H1 + ((br * 32 + (bid & 31)) * 4 + tw) * 8192 + lane * 16 + 1024 * j; d[j] = v2u{0u, 0u}; }
      off[8] = WS_ML + (br * 8 + (tw >> 1) * 4 + ((bid >> 3) & 3)) * 4096 + (4 * (c & 7) + g) * 128 + (bid & 7) * 16;
      d[8] = v2u{0u, 0u};
      if (br == 0) {
        spin(try_wo, IC<16>{});
        wait_ge(W_MCNT + (ALATE ? W_PER : 0), (unsigned)(ALATE ? 1 : NCONS), IC<8>{});
        if (!ALATE) delay(DH1);
        poll(d, off, tA, tB, IC<(ALATE ? 0 : 16)>{}, [] {});
      } else {
        wait_ge(W_MCNT + wb, (unsigned)NCONS, IC<2>{});
        if (!ALATE) st(held, o2 - 128 * 4096);
        poll(d, off, tA, tB, IC<0>{}, [&] { if (!have_wo) try_wo(); });
      }
      if (WX > 0 && br == 0 && tw == 0) {
        float lag = (float)(int)(rfl(ctl[W_STAMP]) - d[8][1]);
        lag += dpp<ROR1>(lag); lag += dpp<ROR2>(lag); lag += dpp<ROR4>(lag);
        const int ticks = (int)f(rfl(u(xrow_sum(lag)))) / 32;
        const int n = ticks > DEAD ? min(ticks * GAIN / 100, 64) : 0;
        ctl[W_WIDE] = (unsigned)(n + 1);
        if constexpr (STAMPS) { if (lane == 0) *(volatile LAS u64*)(L + L_BYTES + 23 * 8) = (u64)n * 100; }
      }
      float M = f(d[8][0]);
      M = fmaxf(M, dpp<ROR1>(M)); M = fmaxf(M, dpp<ROR2>(M)); M = fmaxf(M, dpp<ROR4>(M));
      M = xrow_max(M);
      const float w = __builtin_amdgcn_exp2f(f(d[8][0]) - M);
      float S = w;
      S += dpp<ROR1>(S); S += dpp<ROR2>(S); S += dpp<ROR4>(S);
      const float inv = 1.0f / xrow_sum(S);
      f32x4 o = zf;
#define ACC(J) o += f32x4{f(d[J][0] << 16), f(d[J][0] & 0xffff0000u), f(d[J][1] << 16), f(d[J][1] & 0xffff0000u)} * dpp<BCAST + J>(w);
      ACC(0) ACC(1) ACC(2) ACC(3) ACC(4) ACC(5) ACC(6) ACC(7)
#undef ACC
      const v2u pk = {cvtpk(xrow_sum(o[0]) * inv, xrow_sum(o[1]) * inv), cvtpk(xrow_sum(o[2]) * inv, xrow_sum(o[3]) * inv)};
      if (lane < 16) *(LAS v2u*)(L + L_OIMG + br * 512 + tw * 128 + lane * 8) = pk;
      LDS_WAIT();
      lds_add(W_ORDY + wb);
      if (tw == 0 && br == 1) stamp(1);
      if (!have_wo) spin(try_wo, IC<0>{});
      wait_ge(W_ORDY + wb, (unsigned)NCONS, IC<0>{});
      f32x4 a0 = zf, a1 = zf;
#pragma unroll
      for (int ks = 0; ks < 4; ++ks) {
        const v4u of = *(const LAS v4u*)(L + L_OIMG + br * 512 + (c & 1) * 256 + ks * 64 + g * 16);
        a0 = mfma(wf[2 * ks], of, a0);
        a1 = mfma(wf[2 * ks + 1], of, a1);
      }
      const f32x4 r = (c & 2) ? a1 : a0;
      held = v4u{u((c & 4) ? r[2] : r[0]), tA, u((c & 4) ? r[3] : r[1]), tB};
      if (br == 1 || ALATE) st(held, o2);
      if (tw == 0) stamp(br ? 7 : 22);
    }
    return;
  }

  const int ci = wave - 1;
  v4u xf[4];
#pragma unroll
  for (int ks = 0; ks < 4; ++ks) xf[ks] = *(const GAS v4u*)(p.x[bid >> 7] + (c & 1) * 1024 + ((bid >> 6) & 1) * 512 + ci * 128 + ks * 32 + 8 * g);
  __builtin_amdgcn_s_barrier();
  vmwait<0>();
  {
    const LAS unsigned char* s = L + L_RING + ci * 2 * SLOT + (8 * g + tq) * 128 + tp * 8;
    v4u w0[8], w1[8];
#pragma unroll
    for (int j = 0; j < 8; ++j) {
      const int o = (j >> 2) * 4096 + (j & 3) * 32;
      w0[j] = trfrag(s + o, s + o + 512); w1[j] = trfrag(s + 8192 + o, s + 8192 + o + 512);
    }
    LDS_WAIT();
    if (lane < 2) ctl[W_FREE + 2 * ci + lane] = (unsigned)(2 * ci + lane + 1);
    PIN8(w0); PIN8(w1);
    f32x4 a[4];
#pragma unroll
    for (int e = 0; e < 4; ++e) a[e] = mfma(w1[4 + e], xf[3], mfma(w1[e], xf[2], mfma(w0[4 + e], xf[1], mfma(w0[e], xf[0], zf))));
    const f32x4 r = pick(a[0], a[1], a[2], a[3]);
    if (c < 8) *(LAS f32x4*)(L + L_QRED + ci * 512 + (c & 1) * 256 + (c >> 1) * 64 + g * 16) = r;
    LDS_WAIT();
    lds_add(W_QCNT);
  }
  wait_ge(W_TAGOK, 1u, IC<0>{});
  const unsigned tA = rfl(ctl[W_TAGA]), tB = rfl(ctl[W_TAGB]);
  if (ci == 0) {
    wait_ge(W_QCNT, (unsigned)NCONS, IC<0>{});
    f32x2 q = {0.f, 0.f};
#pragma unroll
    for (int w = 0; w < NCONS; ++w) q += *(const LAS f32x2*)(L + L_QRED + w * 512 + lane * 8);
    st(v4u{u(q[0]), tA, u(q[1]), tB},
       WS_Q + ((((bid >> 7) * 4 + ((bid >> 4) & 3)) * 2 + (lane >> 5)) * 2 + ((bid >> 6) & 1)) * 8192 + (bid & 15) * 512 + (lane & 31) * 16);
    ctl[W_QPUB] = 1u;
  }
  if (ci == 0) stamp(3);

  v4u keep = zero4, keepl = zero4;
#pragma unroll 1
  for (int br = 0; br < 2; ++br) {
    const int wb = W_PER * br, fk = (br ? F_KB : F_KA) + ci, fv = (br ? F_VB : F_VA) + ci;
    f32x4 sc[TPC][2];
    v4u qf[4];
    unsigned kr = (unsigned)c * 256u + (unsigned)((g ^ c) * 16);
    asm volatile("" : "+v"(kr));
    auto ktile = [&](auto tc) {
      constexpr int t = decltype(tc)::value;
      const int fl = fk + NCONS * t, slot = fl & (NSLOT - 1);
      wait_ge(W_FULL + slot, (unsigned)(fl + 1), IC<1>{});
      v4u K[8];
#pragma unroll
      for (int j = 0; j < 8; ++j) K[j] = *(const LAS v4u*)(L + L_RING + ((kr + slot * SLOT) ^ (64u * (j & 3))) + (j >> 2) * 4096);
      LDS_WAIT();
      ctl[W_FREE + slot] = (unsigned)(fl + 1);
      PIN8(K);
      if constexpr (t == 0) {
        wait_ge(W_QRDY + wb, (unsigned)NCONS, IC<1>{});
#pragma unroll
        for (int j = 0; j < 4; ++j) {
          const v4u q = *(const LAS v4u*)(L + L_QIMG + br * 2048 + (c & 7) * 256 + j * 64 + g * 16);
          qf[j] = c < 8 ? q : zero4;
        }
        if (ci == 0 && br == 0) stamp(4);
      }
      f32x4 s0 = zf, s1 = zf;
#pragma unroll
      for (int j = 0; j < 4; ++j) { s0 = mfma(K[j], qf[j], s0); s1 = mfma(K[4 + j], qf[j], s1); }
      sc[t][0] = s0; sc[t][1] = s1;
    };
    each<0, TPC>(ktile);
    {
      float m = -INFINITY;
#pragma unroll
      for (int t = 0; t < TPC; ++t)
#pragma unroll
        for (int j = 0; j < 2; ++j) m = fmaxf(m, fmaxf(fmaxf(sc[t][j][0], sc[t][j][1]), fmaxf(sc[t][j][2], sc[t][j][3])));
      m = xrow_max(m) * C2;
      if (lane < 8) smax[ci * 8 + lane] = m;
      LDS_WAIT();
      lds_add(W_XCNT + wb);
    }
    wait_ge(W_XCNT + wb, (unsigned)NCONS, IC<0>{});
    v4u pf[TPC];
    {
      const int h = c & 7;
      const float m = fmaxf(fmaxf(smax[h], smax[8 + h]), fmaxf(smax[16 + h], smax[24 + h]));
      float l = 0.f;
#pragma unroll
      for (int t = 0; t < TPC; ++t) {
        float pr[8];
#pragma unroll
        for (int i = 0; i < 8; ++i) pr[i] = __builtin_amdgcn_exp2f(fmaf(sc[t][i >> 2][i & 3], C2, -m));
        pf[t] = v4u{cvtpk(pr[0], pr[1]), cvtpk(pr[2], pr[3]), cvtpk(pr[4], pr[5]), cvtpk(pr[6], pr[7])};
        l += ((pr[0] + pr[1]) + (pr[2] + pr[3])) + ((pr[4] + pr[5]) + (pr[6] + pr[7]));
      }
      l = xrow_sum(l);
      if (lane < 8) ssum[ci * 8 + lane] = l;
      LDS_WAIT();
      lds_add(W_LCNT + wb);
    }
    const int h = 2 * ci + (lane >> 5), l5 = lane & 31;
    wait_ge(W_LCNT + wb, (unsigned)NCONS, IC<0>{});
    const float inv = 1.0f / ((ssum[h] + ssum[8 + h]) + (ssum[16 + h] + ssum[24 + h]));
    const int h8 = lane & 7;
    const float lse = fmaxf(fmaxf(smax[h8], smax[8 + h8]), fmaxf(smax[16 + h8], smax[24 + h8]))
                    + __builtin_amdgcn_logf((ssum[h8] + ssum[8 + h8]) + (ssum[16 + h8] + ssum[24 + h8]));
    f32x4 acc[8];
#pragma unroll
    for (int e = 0; e < 8; ++e) acc[e] = zf;
    unsigned vr = (unsigned)(4 * g + tq) * 256u + (unsigned)((4 * (g & 1) + tq) * 32 + tp * 8);
    asm volatile("" : "+v"(vr));
    auto vtile = [&](auto tc) {
      constexpr int t = decltype(tc)::value;
      const int fl = fv + NCONS * t, slot = fl & (NSLOT - 1);
      wait_ge(W_FULL + slot, (unsigned)(fl + 1), IC<1>{});
      if (t == TPC - 1 && ci == 3 && br == 1) stamp(19);
      v4u V[8];
#pragma unroll
      for (int e = 0; e < 8; ++e) { const LAS unsigned char* s = L + L_RING + ((vr + slot * SLOT) ^ (32u * e)); V[e] = trfrag(s, s + 4096); }
      LDS_WAIT();
      ctl[W_FREE + slot] = (unsigned)(fl + 1);
      PIN8(V);
#pragma unroll
      for (int e = 0; e < 8; ++e) acc[e] = mfma(V[e], pf[t], acc[e]);
    };
    each<0, TPC>(vtile);
    if (c < 8) {
      LAS unsigned char* row = L + L_MRG + (ci * 8 + c) * MROW + g * 16;
#pragma unroll
      for (int e = 0; e < 8; ++e) *(LAS f32x4*)(row + e * 64) = acc[e];
    }
    LDS_WAIT();
    lds_add(W_MCNT + wb);
    const int rec = WS_H1 + ((br * 32 + kvh * 8 + h) * 4 + b * 2 + (l5 >> 4)) * 8192 + part * 256 + (l5 & 15) * 16;
    const int mlo = lane < 8 && ci == 0 ? WS_ML + (br * 8 + sidx) * 4096 + part * 128 + lane * 16 : OOB;
    if (ALATE && br == 1) { st(keep, rec - 32 * 4 * 8192); st(keepl, mlo == OOB ? OOB : mlo - 8 * 4096); }
    wait_ge(W_MCNT + wb, (unsigned)NCONS, IC<0>{});
    const LAS unsigned char* mr = L + L_MRG + h * MROW + l5 * 16;
    const f32x4 o = ((*(const LAS f32x4*)mr + *(const LAS f32x4*)(mr + 8 * MROW)) + (*(const LAS f32x4*)(mr + 16 * MROW) + *(const LAS f32x4*)(mr + 24 * MROW))) * inv;
    keep = v4u{cvtpk(o[0], o[1]), tA, cvtpk(o[2], o[3]), tB};
    keepl = v4u{u(lse), tA, (unsigned)rt(), tB};
    if (ci == 0 && br == 0) ctl[W_STAMP] = rfl(keepl[2]);
    if (!ALATE || br == 1) { st(keep, rec); st(keepl, mlo); }
    if (ci == 3) stamp(br ? 6 : 5);
  }
}

template <int WX, int GAIN, int DEAD, int DH1, int STAMPS>
static void launch(void* const* d_in, void* d_out, void* d_ws, hipStream_t stream) {
  Params p;
  for (int br = 0; br < 2; ++br) {
    p.x[br] = (const u16*)d_in[5 * br + 0]; p.k[br] = (const u16*)d_in[5 * br + 1]; p.v[br] = (const u16*)d_in[5 * br + 2];
    p.wq[br] = (const u16*)d_in[5 * br + 3]; p.wo[br] = (const u16*)d_in[5 * br + 4];
  }
  p.out = (u16*)d_out; p.ws = (unsigned char*)d_ws;
  constexpr int bytes = L_BYTES + (STAMPS ? 256 : 0);
  (void)hipFuncSetAttribute((const void*)k_fused<WX, GAIN, DEAD, DH1, STAMPS>, hipFuncAttributeMaxDynamicSharedMemorySize, bytes);
  hipLaunchKernelGGL((k_fused<WX, GAIN, DEAD, DH1, STAMPS>), dim3(NWG), dim3(64 * NWAVE), bytes, stream, p);
}

}

extern "C" void kernel_launch(void* const* d_in, const int* in_sizes, int n_in, void* d_out, int out_size, void* d_ws, size_t ws_size,
                              hipStream_t stream) {
  fused::launch<CFG>(d_in, d_out, d_ws, stream);
}
```

```cpp
#define DK 0
#define DSEL true
#include <hip/hip_runtime.h>
#include <hip/hip_bf16.h>
#include <cstdint>

#define GAS __attribute__((address_space(1)))
#define LAS __attribute__((address_space(3)))
typedef unsigned v4u __attribute__((ext_vector_type(4)));
typedef unsigned v2u __attribute__((ext_vector_type(2)));
typedef float f32x4 __attribute__((ext_vector_type(4)));
typedef float f32x2 __attribute__((ext_vector_type(2)));
typedef short bf16x8 __attribute__((ext_vector_type(8)));
typedef short s16x4 __attribute__((ext_vector_type(4)));
typedef unsigned short u16;
typedef unsigned long long u64;

#define CFG 16, 15, 30, 250, 1

namespace fused {

constexpr int SK = 32768, DH = 128;
constexpr int NWG = 256, NCONS = 4, NWAVE = 10;
constexpr int NSLOT = 16, SLOT = 8192;
constexpr int NT = 32, TPC = NT / NCONS;
constexpr int F_KA = 8, F_VA = F_KA + NT, F_WOA = F_VA + NT, F_KB = F_WOA + 4, F_VB = F_KB + NT, F_WOB = F_VB + NT;
constexpr float C2 = 0.088388347648318440f * 1.4426950408889634f;
constexpr u64 TCAP = 2000000;
constexpr int OOB = 0x7ffffff0;

constexpr int WS_EPOCH = 0;
constexpr int WS_ML = 4096;
constexpr int WS_Q = WS_ML + 2 * 8 * 4096;
constexpr int WS_H2 = WS_Q + 2 * 8 * 16384;
constexpr int WS_H1 = WS_H2 + 256 * 4096;
constexpr int WS_END = WS_H1 + 2 * 128 * 8192;
constexpr int WS_STAMP = WS_END;

constexpr int L_RING = 0;
constexpr int L_CTL = NSLOT * SLOT;
constexpr int L_QIMG = L_CTL + 512;
constexpr int L_QRED = L_QIMG + 4096;
constexpr int L_OIMG = L_QRED + 2048;
constexpr int L_STAT = L_OIMG + 1024;
constexpr int MROW = 528;
constexpr int L_MRG = L_STAT + 512;
constexpr int L_BYTES = L_MRG + 4 * 8 * MROW;
enum { W_FULL = 0, W_FREE = 16, W_ABORT = 32, W_TAGA, W_TAGB, W_TAGOK, W_WIDE, W_STAMP,
       W_QCNT = 40, W_QPUB, W_QRDY, W_XCNT, W_LCNT, W_SRDY, W_MCNT, W_ORDY, W_WCNT, W_PER = 16 };
static_assert(L_BYTES <= 160 * 1024 && L_BYTES > 80 * 1024, "one workgroup per CU");

struct Params { const u16* x[2]; const u16* k[2]; const u16* v[2]; const u16* wq[2]; const u16* wo[2]; u16* out; unsigned char* ws; };

__device__ __forceinline__ u64 rt() { return __builtin_amdgcn_s_memrealtime(); }
__device__ __forceinline__ unsigned rfl(unsigned v) { return (unsigned)__builtin_amdgcn_readfirstlane((int)v); }
__device__ __forceinline__ unsigned cvtpk(float lo, float hi) {
  unsigned r; asm("v_cvt_pk_bf16_f32 %0, %1, %2" : "=v"(r) : "v"(lo), "v"(hi)); return r;
}
__device__ __forceinline__ f32x4 mfma(v4u a, v4u b, f32x4 c) {
  return __builtin_amdgcn_mfma_f32_16x16x32_bf16(__builtin_bit_cast(bf16x8, a), __builtin_bit_cast(bf16x8, b), c, 0, 0, 0);
}
__device__ __forceinline__ float f(unsigned u) { return __uint_as_float(u); }
__device__ __forceinline__ unsigned u(float x) { return __float_as_uint(x); }
__device__ __forceinline__ float xrow_max(float x) {
  auto s = __builtin_amdgcn_permlane16_swap(u(x), u(x), false, false);
  x = fmaxf(f(s[0]), f(s[1]));
  auto t = __builtin_amdgcn_permlane32_swap(u(x), u(x), false, false);
  return fmaxf(f(t[0]), f(t[1]));
}
__device__ __forceinline__ float xrow_sum(float x) {
  auto s = __builtin_amdgcn_permlane16_swap(u(x), u(x), false, false);
  x = f(s[0]) + f(s[1]);
  auto t = __builtin_amdgcn_permlane32_swap(u(x), u(x), false, false);
  return f(t[0]) + f(t[1]);
}
template <int CTRL> __device__ __forceinline__ float dpp(float x) {
  return __builtin_bit_cast(float, __builtin_amdgcn_mov_dpp(__builtin_bit_cast(int, x), CTRL, 0xf, 0xf, true));
}
constexpr int ROR1 = 0x121, ROR2 = 0x122, ROR4 = 0x124, ROR8 = 0x128, BCAST = 0x150;
template <int N> struct IC { static constexpr int value = N; };
template <int I, int E, class F> __device__ __forceinline__ void each(F& fn) {
  if constexpr (I < E) { fn(IC<I>{}); each<I + 1, E>(fn); }
}
template <int N> __device__ __forceinline__ void vmwait() { asm volatile("s_waitcnt vmcnt(%0)" ::"i"(N) : "memory"); }
#define LDS_WAIT() asm volatile("s_waitcnt lgkmcnt(0)" ::: "memory")
#define LDS_ACQ() __builtin_amdgcn_fence(__ATOMIC_ACQUIRE, "workgroup")
#define SCHED() __builtin_amdgcn_sched_barrier(0)
#define PIN8(w) asm volatile("" : "+v"(w[0]), "+v"(w[1]), "+v"(w[2]), "+v"(w[3]), "+v"(w[4]), "+v"(w[5]), "+v"(w[6]), "+v"(w[7]) : : "memory")
#define GLDS(src, dst, imm) __builtin_amdgcn_global_load_lds((const GAS unsigned*)(src), (LAS unsigned*)(dst), 16, (imm), 2)

__device__ __forceinline__ v4u trfrag(const LAS unsigned char* lo, const LAS unsigned char* hi) {
  const v2u a = __builtin_bit_cast(v2u, __builtin_amdgcn_ds_read_tr16_b64_v4i16((LAS s16x4*)lo));
  const v2u b = __builtin_bit_cast(v2u, __builtin_amdgcn_ds_read_tr16_b64_v4i16((LAS s16x4*)hi));
  return v4u{a[0], a[1], b[0], b[1]};
}

template <int WX, int GAIN, int DEAD, int DH1, int STAMPS>
__global__ void __launch_bounds__(64 * NWAVE) k_fused(Params p) {
  constexpr int DQB = 600, QDELAY = 100, RT = 5, STAG = 0, WN = 16, HALF = 0, ALATE = 0;
  static_assert(WN % 4 == 0 && (HALF || WN % 8 == 0) && WN >= 8 && WN <= 48 && RT >= 0 && RT <= 5, "pieces left in flight; run of 2^RT tiles");
  extern __shared__ __attribute__((aligned(16))) unsigned char lds_raw[];
  LAS unsigned char* L = (LAS unsigned char*)lds_raw;
  volatile LAS unsigned* ctl = (volatile LAS unsigned*)(L + L_CTL);
  const u64 t_start = rt();
  const int bid = blockIdx.x, sidx = bid >> 5, part = bid & 31, b = sidx >> 2, kvh = sidx & 3;
  const int tid = threadIdx.x, lane = tid & 63, wave = (int)rfl(tid >> 6);
  const int c = lane & 15, g = lane >> 4;
  if (__builtin_expect(wave >= 1 && wave <= NCONS, 1)) {
    const unsigned char* wq = (const unsigned char*)p.wq[bid >> 7] + (size_t)(((bid >> 6) & 1) * 512 + (wave - 1) * 128) * 8192 + (bid & 63) * 128;
    const unsigned wq_off = (unsigned)(lane >> 3) * 8192u + (unsigned)(lane & 7) * 16u;
#pragma unroll
    for (int j = 0; j < 16; ++j) GLDS(wq + (size_t)j * 8 * 8192 + wq_off, L + L_RING + (wave - 1) * 2 * SLOT + j * 1024, 0);
  }
  u64* stamps = (u64*)(p.ws + WS_STAMP) + bid * 32;
  auto stamp = [&](int i) { if constexpr (STAMPS) { const u64 t = rt(); if (lane == 0) *(volatile LAS u64*)(L + L_BYTES + i * 8) = t; } };

  auto spin = [&](auto&& cond, auto nap) {
    for (unsigned it = 1; !cond(); ++it) {
      if constexpr (decltype(nap)::value > 0) __builtin_amdgcn_s_sleep(decltype(nap)::value);
      if ((it & 63u) == 0u) {
        if (rt() - t_start > TCAP) ctl[W_ABORT] = 1u;
        if (rfl(ctl[W_ABORT])) break;
      }
    }
  };
  auto wait_ge = [&](int w, unsigned n, auto nap) {
    spin([&] { return rfl(ctl[w]) >= n; }, nap);
    LDS_ACQ();
  };

  if (__builtin_expect(wave == 0, 1)) {
    __builtin_amdgcn_s_setprio(3);
    ctl[lane] = 0u; ctl[64 + lane] = 0u;
    unsigned koff[4], voff[2];
#pragma unroll
    for (int j = 0; j < 4; ++j) koff[j] = (unsigned)g * 256u + (unsigned)((c ^ (4 * j + g)) * 16);
#pragma unroll
    for (int j = 0; j < 2; ++j) voff[j] = (unsigned)g * 256u + (unsigned)((c ^ (2 * (4 * j + g))) * 16);
    constexpr int LAG = WN / 8 + 1;
    int fill = F_KA, lastpark = 0;
    u64 parked = 0;
    unsigned fr = 0u;
    auto full = [&](int fl) { ctl[W_FULL + (fl & (NSLOT - 1))] = (unsigned)(fl + 1); };
    auto drain = [&]() {
      vmwait<24>(); full(fill - 4); vmwait<16>(); full(fill - 3); vmwait<8>(); full(fill - 2); vmwait<0>(); full(fill - 1);
#pragma unroll
      for (int j = 5; j <= LAG + WX / 8; ++j) full(fill - j);
    };
    auto room = [&]() {
      if (rfl(fr) < (unsigned)(fill - NSLOT + 1) && fill >= NSLOT) {
        const u64 tp = rt();
        drain();
        spin([&] { return rfl(ctl[W_FREE + (fill & (NSLOT - 1))]) >= (unsigned)(fill - NSLOT + 1); }, IC<1>{});
        parked += rt() - tp; lastpark = fill;
      }
    };
    unsigned wd = 0u;
    int until = -1;
    auto issue = [&](const unsigned char* s0, const unsigned char* s1, unsigned o0, unsigned o1, unsigned o2, unsigned o3) {
      const unsigned dst = (unsigned)(size_t)(L + L_RING) + (unsigned)((fill & (NSLOT - 1)) * SLOT);
      room();
      if (WX > 0 && until < 0 && rfl(wd) != 0u) until = fill + (int)rfl(wd) - 1;
#define FILL(N) asm volatile("s_mov_b32 m0, %0\n\ts_waitcnt vmcnt(%7)\n\t"                                                     \
                   "global_load_lds_dwordx4 %3, %1 nt\n\tglobal_load_lds_dwordx4 %4, %1 offset:1024 nt\n\t"                    \
                   "global_load_lds_dwordx4 %5, %1 offset:2048 nt\n\tglobal_load_lds_dwordx4 %6, %1 offset:3072 nt\n\t"        \
                   "s_add_u32 m0, %0, 0x1000\n\ts_nop 0\n\t"                                                                  \
                   "global_load_lds_dwordx4 %3, %2 nt\n\tglobal_load_lds_dwordx4 %4, %2 offset:1024 nt\n\t"                    \
                   "global_load_lds_dwordx4 %5, %2 offset:2048 nt\n\tglobal_load_lds_dwordx4 %6, %2 offset:3072 nt"            \
                   :: "s"(dst), "s"(s0), "s"(s1), "v"(o0), "v"(o1), "v"(o2), "v"(o3), "i"(N) : "memory", "scc")
      if (WX > 0 && fill < until) {
        FILL(WN + WX);
        full(fill - LAG - WX / 8);
      } else {
        FILL(WN);
#pragma unroll
        for (int j = WX / 8; j >= 0; --j) full(fill - LAG - j);
      }
#undef FILL
      ++fill;
      fr = ctl[W_FREE + (fill & (NSLOT - 1))];
      if (WX > 0) wd = ctl[W_WIDE];
    };
    const size_t sbase = (size_t)sidx * SK * DH * 2;
    auto tile_off = [&](int i) -> size_t {
      constexpr int NR = NT >> RT;
      return (size_t)((((((i >> RT) + STAG * sidx) & (NR - 1)) * 32 + part) << RT) | (i & ((1 << RT) - 1))) * 8192;
    };
    const size_t wo_base = (size_t)((bid & 31) * 128) * 2048 + (bid >> 5) * 256;
    const unsigned woff = (unsigned)g * 2048u + (unsigned)c * 16u;
    LDS_WAIT();
    __builtin_amdgcn_s_barrier();
    stamp(11);
#pragma unroll 1
    for (int br = 0; br < 2; ++br) {
      const unsigned char* kb = (const unsigned char*)p.k[br] + sbase;
      const unsigned char* vb = (const unsigned char*)p.v[br] + sbase;
#pragma unroll 1
      for (int i = 0; i < NT; ++i) { const unsigned char* s = kb + tile_off(i); issue(s, s + 4096, koff[0], koff[1], koff[2], koff[3]); }
      stamp(24 + 2 * br);
#pragma unroll 1
      for (int i = 0; i < NT; ++i) { const unsigned char* s = vb + tile_off(i); issue(s, s + 4096, voff[0], voff[1], voff[0], voff[1]); }
      stamp(25 + 2 * br);
#pragma unroll 1
      for (int i = 0; i < 4; ++i) {
        const unsigned char* s = (const unsigned char*)p.wo[br] + wo_base + (size_t)(i * 32) * 2048;
        issue(s, s + 4 * 8192, woff, woff + 7168u, woff + 14336u, woff + 21504u);
      }
    }
    drain();
    stamp(9);
    if constexpr (STAMPS) { if (lane == 0) { *(volatile LAS u64*)(L + L_BYTES + 20 * 8) = parked; *(volatile LAS u64*)(L + L_BYTES + 21 * 8) = (u64)lastpark * 100; } }
    return;
  }

  const __amdgpu_buffer_rsrc_t rs = __builtin_amdgcn_make_buffer_rsrc(p.ws, 0, WS_END, 0x00020000);
  auto ld = [&](int off) -> v4u { return __builtin_amdgcn_raw_buffer_load_b128(rs, off, 0, 16); };
  auto st = [&](v4u v, int off) { __builtin_amdgcn_raw_buffer_store_b128(v, rs, off, 0, 16); };
  auto lds_add = [&](int w) { if (lane == 0) __hip_atomic_fetch_add((LAS unsigned*)&ctl[w], 1u, __ATOMIC_RELAXED, __HIP_MEMORY_SCOPE_WORKGROUP); };
  auto delay = [&](u64 ticks) { const u64 t0 = rt(); while (rt() - t0 < ticks) __builtin_amdgcn_s_sleep(2); };
  auto poll = [&](auto& v, auto& off, unsigned tA, unsigned tB, auto nap, auto&& shadow) {
    constexpr int N = sizeof(v) / sizeof(v[0]);
    spin([&] {
      v4u t[N];
#pragma unroll
      for (int j = 0; j < N; ++j) t[j] = ld(off[j]);
      SCHED();
      shadow();
      SCHED();
      bool pend = false;
#pragma unroll
      for (int j = 0; j < N; ++j) {
        const bool hit = (t[j][1] == tA) & (t[j][3] == tB) & (off[j] != OOB);
        v[j][0] = hit ? t[j][0] : v[j][0];
        v[j][1] = hit ? t[j][2] : v[j][1];
        off[j] = hit ? OOB : off[j];
        pend = pend | (off[j] != OOB);
      }
      return __any(pend) == 0;
    }, nap);
  };
  auto gather = [&](auto& v, int base, unsigned tA, unsigned tB, auto nap, auto&& shadow) {
    constexpr int N = sizeof(v) / sizeof(v[0]);
    int off[N];
#pragma unroll
    for (int j = 0; j < N; ++j) { off[j] = base + lane * 16 + 1024 * j; v[j] = v2u{0u, 0u}; }
    poll(v, off, tA, tB, nap, shadow);
  };
  const int tq = (lane >> 2) & 3, tp = lane & 3;
  const v4u zero4 = {0u, 0u, 0u, 0u};
  const f32x4 zf = {0.f, 0.f, 0.f, 0.f};
  auto pick = [&](f32x4 a0, f32x4 a1, f32x4 a2, f32x4 a3) -> f32x4 {
    const f32x4 lo = (c & 2) ? a1 : a0, hi = (c & 2) ? a3 : a2;
    return (c & 4) ? hi : lo;
  };
  LAS float* smax = (LAS float*)(L + L_STAT);
  LAS float* ssum = smax + 32;

  if (wave == NCONS + 1) {
    const u64 ep = __hip_atomic_load((GAS u64*)(p.ws + WS_EPOCH), __ATOMIC_RELAXED, __HIP_MEMORY_SCOPE_AGENT);
    __builtin_amdgcn_s_barrier();
    const unsigned tA = rfl((unsigned)ep) + 1u, tB = rfl((unsigned)(ep >> 32)) + 1u;
    ctl[W_TAGA] = tA; ctl[W_TAGB] = tB;
    LDS_WAIT();
    ctl[W_TAGOK] = 1u;
    auto reduce = [&](auto nap) {
      v2u r[4];
      gather(r, WS_H2 + ((bid & 1) * 128 + (bid >> 1)) * 4096, tA, tB, nap, [] {});
      float s0 = (f(r[0][0]) + f(r[1][0])) + (f(r[2][0]) + f(r[3][0])), s1 = (f(r[0][1]) + f(r[1][1])) + (f(r[2][1]) + f(r[3][1]));
      s0 += dpp<ROR8>(s0); s1 += dpp<ROR8>(s1);
      s0 = xrow_sum(s0); s1 = xrow_sum(s1);
      if (lane < 8) {
        const int col = 32 * (bid >> 3) + 16 * ((lane >> 1) & 1) + 4 * ((bid >> 1) & 3) + 2 * (lane >> 2);
        *(GAS unsigned*)(p.out + (lane & 1) * 2048 + (bid & 1) * 1024 + col) = cvtpk(s0, s1);
      }
    };
    wait_ge(W_ORDY + W_PER, (unsigned)NCONS, IC<2>{});
    stamp(17);
    reduce(IC<0>{});
    if (bid == 1 && lane == 0)
      __hip_atomic_store((GAS u64*)(p.ws + WS_EPOCH), ((u64)tB << 32) | tA, __ATOMIC_RELAXED, __HIP_MEMORY_SCOPE_AGENT);
    if constexpr (STAMPS) {
      stamp(18);
      delay(300);
      u64 stat = DK == 0 ? 0ull : *(volatile LAS u64*)(L + L_BYTES + DK * 8) - t_start;
      stat = ((u64)rfl((unsigned)(stat >> 32)) << 32) | rfl((unsigned)stat);
      if (stat > 20000ull || !(DSEL)) stat = 0ull;
      while (rt() - t_start < 10000ull + 4ull * stat) __builtin_amdgcn_s_sleep(2);
    }
    return;
  }

  if (wave > NCONS + 1) {
    const int tw = wave - NCONS - 2;
    __builtin_amdgcn_s_barrier();
    wait_ge(W_QPUB, 1u, IC<2>{});
    const unsigned tA = rfl(ctl[W_TAGA]), tB = rfl(ctl[W_TAGB]);
#pragma unroll 1
    for (int br = 0; br < 2; ++br) {
      delay(br ? DQB : QDELAY);
      v2u q[4];
      int off[4];
      const int qb = WS_Q + ((br * 4 + kvh) * 2 + b) * 16384 + tw * 2048 + lane * 16;
#pragma unroll
      for (int j = 0; j < 4; ++j) { off[j] = qb + (j >> 1) * 8192 + (j & 1) * 1024; q[j] = v2u{0u, 0u}; }
      poll(q, off, tA, tB, IC<4>{}, [] {});
      LAS unsigned* qi = (LAS unsigned*)(L + L_QIMG + br * 2048 + tw * 512 + lane * 4);
      qi[0] = cvtpk(f(q[0][0]) + f(q[2][0]), f(q[0][1]) + f(q[2][1]));
      qi[64] = cvtpk(f(q[1][0]) + f(q[3][0]), f(q[1][1]) + f(q[3][1]));
      LDS_WAIT();
      lds_add(W_QRDY + W_PER * br);
      if (tw == 0) stamp(15 + br);
    }
    v4u held = zero4;
#pragma unroll 1
    for (int br = 0; br < 2; ++br) {
      const int wb = W_PER * br, f0 = br ? F_WOB : F_WOA;
      const int o2 = c < 8 ? WS_H2 + (br * 128 + ((bid >> 5) * 4 + tw) * 4 + g) * 4096 + (bid & 31) * 128 + c * 16 : OOB;
      v4u wf[8];
      bool have_wo = false;
      auto try_wo = [&]() -> bool {
        if (rfl(ctl[W_FULL + ((f0 + 3) & (NSLOT - 1))]) < (unsigned)(f0 + 4)) return false;
        LDS_ACQ();
        const LAS unsigned char* s = L + L_RING + (f0 & (NSLOT - 1)) * SLOT + (8 * g + tq) * 256 + tw * 64 + tp * 8;
#pragma unroll
        for (int j = 0; j < 8; ++j) wf[j] = trfrag(s + (j >> 1) * 8192 + (j & 1) * 32, s + (j >> 1) * 8192 + (j & 1) * 32 + 1024);
        LDS_WAIT();
        PIN8(wf);
        unsigned old = 0u;
        if (lane == 0) old = __hip_atomic_fetch_add((LAS unsigned*)&ctl[W_WCNT + wb], 1u, __ATOMIC_RELAXED, __HIP_MEMORY_SCOPE_WORKGROUP);
        if (rfl(old) == 3u && lane < 4) ctl[W_FREE + ((f0 + lane) & (NSLOT - 1))] = (unsigned)(f0 + lane + 1);
        have_wo = true;
        return true;
      };
      v2u d[9];
      int off[9];
#pragma unroll
      for (int j = 0; j < 8; ++j) { off[j] = WS_H1 + ((br * 32 + (bid & 31)) * 4 + tw) * 8192 + lane * 16 + 1024 * j; d[j] = v2u{0u, 0u}; }
      off[8] = WS_ML + (br * 8 + (tw >> 1) * 4 + ((bid >> 3) & 3)) * 4096 + (4 * (c & 7) + g) * 128 + (bid & 7) * 16;
      d[8] = v2u{0u, 0u};
      if (br == 0) {
        spin(try_wo, IC<16>{});
        wait_ge(W_MCNT + (ALATE ? W_PER : 0), (unsigned)(ALATE ? 1 : NCONS), IC<8>{});
        if (!ALATE) delay(DH1);
        poll(d, off, tA, tB, IC<(ALATE ? 0 : 16)>{}, [] {});
      } else {
        wait_ge(W_MCNT + wb, (unsigned)NCONS, IC<2>{});
        if (!ALATE) st(held, o2 - 128 * 4096);
        poll(d, off, tA, tB, IC<0>{}, [&] { if (!have_wo) try_wo(); });
      }
      if (WX > 0 && br == 0 && tw == 0) {
        float lag = (float)(int)(rfl(ctl[W_STAMP]) - d[8][1]);
        lag += dpp<ROR1>(lag); lag += dpp<ROR2>(lag); lag += dpp<ROR4>(lag);
        const int ticks = (int)f(rfl(u(xrow_sum(lag)))) / 32;
        const int n = ticks > DEAD ? min(ticks * GAIN / 100, 64) : 0;
        ctl[W_WIDE] = (unsigned)(n + 1);
        if constexpr (STAMPS) { if (lane == 0) *(volatile LAS u64*)(L + L_BYTES + 23 * 8) = (u64)n * 100; }
      }
      float M = f(d[8][0]);
      M = fmaxf(M, dpp<ROR1>(M)); M = fmaxf(M, dpp<ROR2>(M)); M = fmaxf(M, dpp<ROR4>(M));
      M = xrow_max(M);
      const float w = __builtin_amdgcn_exp2f(f(d[8][0]) - M);
      float S = w;
      S += dpp<ROR1>(S); S += dpp<ROR2>(S); S += dpp<ROR4>(S);
      const float inv = 1.0f / xrow_sum(S);
      f32x4 o = zf;
#define ACC(J) o += f32x4{f(d[J][0] << 16), f(d[J][0] & 0xffff0000u), f(d[J][1] << 16), f(d[J][1] & 0xffff0000u)} * dpp<BCAST + J>(w);
      ACC(0) ACC(1) ACC(2) ACC(3) ACC(4) ACC(5) ACC(6) ACC(7)
#undef ACC
      const v2u pk = {cvtpk(xrow_sum(o[0]) * inv, xrow_sum(o[1]) * inv), cvtpk(xrow_sum(o[2]) * inv, xrow_sum(o[3]) * inv)};
      if (lane < 16) *(LAS v2u*)(L + L_OIMG + br * 512 + tw * 128 + lane * 8) = pk;
      LDS_WAIT();
      lds_add(W_ORDY + wb);
      if (tw == 0 && br == 1) stamp(1);
      if (!have_wo) spin(try_wo, IC<0>{});
      wait_ge(W_ORDY + wb, (unsigned)NCONS, IC<0>{});
      f32x4 a0 = zf, a1 = zf;
#pragma unroll
      for (int ks = 0; ks < 4; ++ks) {
        const v4u of = *(const LAS v4u*)(L + L_OIMG + br * 512 + (c & 1) * 256 + ks * 64 + g * 16);
        a0 = mfma(wf[2 * ks], of, a0);
        a1 = mfma(wf[2 * ks + 1], of, a1);
      }
      const f32x4 r = (c & 2) ? a1 : a0;
      held = v4u{u((c & 4) ? r[2] : r[0]), tA, u((c & 4) ? r[3] : r[1]), tB};
      if (br == 1 || ALATE) st(held, o2);
      if (tw == 0) stamp(br ? 7 : 22);
    }
    return;
  }

  const int ci = wave - 1;
  v4u xf[4];
#pragma unroll
  for (int ks = 0; ks < 4; ++ks) xf[ks] = *(const GAS v4u*)(p.x[bid >> 7] + (c & 1) * 1024 + ((bid >> 6) & 1) * 512 + ci * 128 + ks * 32 + 8 * g);
  __builtin_amdgcn_s_barrier();
  vmwait<0>();
  {
    const LAS unsigned char* s = L + L_RING + ci * 2 * SLOT + (8 * g + tq) * 128 + tp * 8;
    v4u w0[8], w1[8];
#pragma unroll
    for (int j = 0; j < 8; ++j) {
      const int o = (j >> 2) * 4096 + (j & 3) * 32;
      w0[j] = trfrag(s + o, s + o + 512); w1[j] = trfrag(s + 8192 + o, s + 8192 + o + 512);
    }
    LDS_WAIT();
    if (lane < 2) ctl[W_FREE + 2 * ci + lane] = (unsigned)(2 * ci + lane + 1);
    PIN8(w0); PIN8(w1);
    f32x4 a[4];
#pragma unroll
    for (int e = 0; e < 4; ++e) a[e] = mfma(w1[4 + e], xf[3], mfma(w1[e], xf[2], mfma(w0[4 + e], xf[1], mfma(w0[e], xf[0], zf))));
    const f32x4 r = pick(a[0], a[1], a[2], a[3]);
    if (c < 8) *(LAS f32x4*)(L + L_QRED + ci * 512 + (c & 1) * 256 + (c >> 1) * 64 + g * 16) = r;
    LDS_WAIT();
    lds_add(W_QCNT);
  }
  wait_ge(W_TAGOK, 1u, IC<0>{});
  const unsigned tA = rfl(ctl[W_TAGA]), tB = rfl(ctl[W_TAGB]);
  if (ci == 0) {
    wait_ge(W_QCNT, (unsigned)NCONS, IC<0>{});
    f32x2 q = {0.f, 0.f};
#pragma unroll
    for (int w = 0; w < NCONS; ++w) q += *(const LAS f32x2*)(L + L_QRED + w * 512 + lane * 8);
    st(v4u{u(q[0]), tA, u(q[1]), tB},
       WS_Q + ((((bid >> 7) * 4 + ((bid >> 4) & 3)) * 2 + (lane >> 5)) * 2 + ((bid >> 6) & 1)) * 8192 + (bid & 15) * 512 + (lane & 31) * 16);
    ctl[W_QPUB] = 1u;
  }
  if (ci == 0) stamp(3);

  v4u keep = zero4, keepl = zero4;
#pragma unroll 1
  for (int br = 0; br < 2; ++br) {
    const int wb = W_PER * br, fk = (br ? F_KB : F_KA) + ci, fv = (br ? F_VB : F_VA) + ci;
    f32x4 sc[TPC][2];
    v4u qf[4];
    unsigned kr = (unsigned)c * 256u + (unsigned)((g ^ c) * 16);
    asm volatile("" : "+v"(kr));
    auto ktile = [&](auto tc) {
      constexpr int t = decltype(tc)::value;
      const int fl = fk + NCONS * t, slot = fl & (NSLOT - 1);
      wait_ge(W_FULL + slot, (unsigned)(fl + 1), IC<1>{});
      v4u K[8];
#pragma unroll
      for (int j = 0; j < 8; ++j) K[j] = *(const LAS v4u*)(L + L_RING + ((kr + slot * SLOT) ^ (64u * (j & 3))) + (j >> 2) * 4096);
      LDS_WAIT();
      ctl[W_FREE + slot] = (unsigned)(fl + 1);
      PIN8(K);
      if constexpr (t == 0) {
        wait_ge(W_QRDY + wb, (unsigned)NCONS, IC<1>{});
#pragma unroll
        for (int j = 0; j < 4; ++j) {
          const v4u q = *(const LAS v4u*)(L + L_QIMG + br * 2048 + (c & 7) * 256 + j * 64 + g * 16);
          qf[j] = c < 8 ? q : zero4;
        }
        if (ci == 0 && br == 0) stamp(4);
      }
      f32x4 s0 = zf, s1 = zf;
#pragma unroll
      for (int j = 0; j < 4; ++j) { s0 = mfma(K[j], qf[j], s0); s1 = mfma(K[4 + j], qf[j], s1); }
      sc[t][0] = s0; sc[t][1] = s1;
    };
    each<0, TPC>(ktile);
    {
      float m = -INFINITY;
#pragma unroll
      for (int t = 0; t < TPC; ++t)
#pragma unroll
        for (int j = 0; j < 2; ++j) m = fmaxf(m, fmaxf(fmaxf(sc[t][j][0], sc[t][j][1]), fmaxf(sc[t][j][2], sc[t][j][3])));
      m = xrow_max(m) * C2;
      if (lane < 8) smax[ci * 8 + lane] = m;
      LDS_WAIT();
      lds_add(W_XCNT + wb);
    }
    wait_ge(W_XCNT + wb, (unsigned)NCONS, IC<0>{});
    v4u pf[TPC];
    {
      const int h = c & 7;
      const float m = fmaxf(fmaxf(smax[h], smax[8 + h]), fmaxf(smax[16 + h], smax[24 + h]));
      float l = 0.f;
#pragma unroll
      for (int t = 0; t < TPC; ++t) {
        float pr[8];
#pragma unroll
        for (int i = 0; i < 8; ++i) pr[i] = __builtin_amdgcn_exp2f(fmaf(sc[t][i >> 2][i & 3], C2, -m));
        pf[t] = v4u{cvtpk(pr[0], pr[1]), cvtpk(pr[2], pr[3]), cvtpk(pr[4], pr[5]), cvtpk(pr[6], pr[7])};
        l += ((pr[0] + pr[1]) + (pr[2] + pr[3])) + ((pr[4] + pr[5]) + (pr[6] + pr[7]));
      }
      l = xrow_sum(l);
      if (lane < 8) ssum[ci * 8 + lane] = l;
      LDS_WAIT();
      lds_add(W_LCNT + wb);
    }
    const int h = 2 * ci + (lane >> 5), l5 = lane & 31;
    wait_ge(W_LCNT + wb, (unsigned)NCONS, IC<0>{});
    const float inv = 1.0f / ((ssum[h] + ssum[8 + h]) + (ssum[16 + h] + ssum[24 + h]));
    const int h8 = lane & 7;
    const float lse = fmaxf(fmaxf(smax[h8], smax[8 + h8]), fmaxf(smax[16 + h8], smax[24 + h8]))
                    + __builtin_amdgcn_logf((ssum[h8] + ssum[8 + h8]) + (ssum[16 + h8] + ssum[24 + h8]));
    f32x4 acc[8];
#pragma unroll
    for (int e = 0; e < 8; ++e) acc[e] = zf;
    unsigned vr = (unsigned)(4 * g + tq) * 256u + (unsigned)((4 * (g & 1) + tq) * 32 + tp * 8);
    asm volatile("" : "+v"(vr));
    auto vtile = [&](auto tc) {
      constexpr int t = decltype(tc)::value;
      const int fl = fv + NCONS * t, slot = fl & (NSLOT - 1);
      wait_ge(W_FULL + slot, (unsigned)(fl + 1), IC<1>{});
      if (t == TPC - 1 && ci == 3 && br == 1) stamp(19);
      v4u V[8];
#pragma unroll
      for (int e = 0; e < 8; ++e) { const LAS unsigned char* s = L + L_RING + ((vr + slot * SLOT) ^ (32u * e)); V[e] = trfrag(s, s + 4096); }
      LDS_WAIT();
      ctl[W_FREE + slot] = (unsigned)(fl + 1);
      PIN8(V);
#pragma unroll
      for (int e = 0; e < 8; ++e) acc[e] = mfma(V[e], pf[t], acc[e]);
    };
    each<0, TPC>(vtile);
    if (c < 8) {
      LAS unsigned char* row = L + L_MRG + (ci * 8 + c) * MROW + g * 16;
#pragma unroll
      for (int e = 0; e < 8; ++e) *(LAS f32x4*)(row + e * 64) = acc[e];
    }
    LDS_WAIT();
    lds_add(W_MCNT + wb);
    const int rec = WS_H1 + ((br * 32 + kvh * 8 + h) * 4 + b * 2 + (l5 >> 4)) * 8192 + part * 256 + (l5 & 15) * 16;
    const int mlo = lane < 8 && ci == 0 ? WS_ML + (br * 8 + sidx) * 4096 + part * 128 + lane * 16 : OOB;
    if (ALATE && br == 1) { st(keep, rec - 32 * 4 * 8192); st(keepl, mlo == OOB ? OOB : mlo - 8 * 4096); }
    wait_ge(W_MCNT + wb, (unsigned)NCONS, IC<0>{});
    const LAS unsigned char* mr = L + L_MRG + h * MROW + l5 * 16;
    const f32x4 o = ((*(const LAS f32x4*)mr + *(const LAS f32x4*)(mr + 8 * MROW)) + (*(const LAS f32x4*)(mr + 16 * MROW) + *(const LAS f32x4*)(mr + 24 * MROW))) * inv;
    keep = v4u{cvtpk(o[0], o[1]), tA, cvtpk(o[2], o[3]), tB};
    keepl = v4u{u(lse), tA, (unsigned)rt(), tB};
    if (ci == 0 && br == 0) ctl[W_STAMP] = rfl(keepl[2]);
    if (!ALATE || br == 1) { st(keep, rec); st(keepl, mlo); }
    if (ci == 3) stamp(br ? 6 : 5);
  }
}

template <int WX, int GAIN, int DEAD, int DH1, int STAMPS>
static void launch(void* const* d_in, void* d_out, void* d_ws, hipStream_t stream) {
  Params p;
  for (int br = 0; br < 2; ++br) {
    p.x[br] = (const u16*)d_in[5 * br + 0]; p.k[br] = (const u16*)d_in[5 * br + 1]; p.v[br] = (const u16*)d_in[5 * br + 2];
    p.wq[br] = (const u16*)d_in[5 * br + 3]; p.wo[br] = (const u16*)d_in[5 * br + 4];
  }
  p.out = (u16*)d_out; p.ws = (unsigned char*)d_ws;
  constexpr int bytes = L_BYTES + (STAMPS ? 256 : 0);
  (void)hipFuncSetAttribute((const void*)k_fused<WX, GAIN, DEAD, DH1, STAMPS>, hipFuncAttributeMaxDynamicSharedMemorySize, bytes);
  hipLaunchKernelGGL((k_fused<WX, GAIN, DEAD, DH1, STAMPS>), dim3(NWG), dim3(64 * NWAVE), bytes, stream, p);
}

}

extern "C" void kernel_launch(void* const* d_in, const int* in_sizes, int n_in, void* d_out, int out_size, void* d_ws, size_t ws_size,
                              hipStream_t stream) {
  fused::launch<CFG>(d_in, d_out, d_ws, stream);
}
```

```cpp
#include <hip/hip_runtime.h>
#include <hip/hip_bf16.h>
#include <cstdint>

#define GAS __attribute__((address_space(1)))
#define LAS __attribute__((address_space(3)))
typedef unsigned v4u __attribute__((ext_vector_type(4)));
typedef unsigned v2u __attribute__((ext_vector_type(2)));
typedef float f32x4 __attribute__((ext_vector_type(4)));
typedef float f32x2 __attribute__((ext_vector_type(2)));
typedef short bf16x8 __attribute__((ext_vector_type(8)));
typedef short s16x4 __attribute__((ext_vector_type(4)));
typedef unsigned short u16;
typedef unsigned long long u64;

#define CFG 16, 15, 30, 250, 0

namespace fused {

constexpr int SK = 32768, DH = 128;
constexpr int NWG = 256, NCONS = 4, NWAVE = 10;
constexpr int NSLOT = 16, SLOT = 8192;
constexpr int NT = 32, TPC = NT / NCONS;
constexpr int F_KA = 8, F_VA = F_KA + NT, F_WOA = F_VA + NT, F_KB = F_WOA + 4, F_VB = F_KB + NT, F_WOB = F_VB + NT;
constexpr float C2 = 0.088388347648318440f * 1.4426950408889634f;
constexpr u64 TCAP = 2000000;
constexpr int OOB = 0x7ffffff0;

constexpr int WS_EPOCH = 0;
constexpr int WS_ML = 4096;
constexpr int WS_Q = WS_ML + 2 * 8 * 4096;
constexpr int WS_H2 = WS_Q + 2 * 8 * 16384;
constexpr int WS_H1 = WS_H2 + 256 * 4096;
constexpr int WS_END = WS_H1 + 2 * 128 * 8192;
constexpr int WS_STAMP = WS_END;

constexpr int L_RING = 0;
constexpr int L_CTL = NSLOT * SLOT;
constexpr int L_QIMG = L_CTL + 512;
constexpr int L_QRED = L_QIMG + 4096;
constexpr int L_OIMG = L_QRED + 2048;
constexpr int L_STAT = L_OIMG + 1024;
constexpr int MROW = 528;
constexpr int L_MRG = L_STAT + 512;
constexpr int L_BYTES = L_MRG + 4 * 8 * MROW;
enum { W_FULL = 0, W_FREE = 16, W_ABORT = 32, W_TAGA, W_TAGB, W_TAGOK, W_WIDE, W_STAMP,
       W_QCNT = 40, W_QPUB, W_QRDY, W_XCNT, W_LCNT, W_SRDY, W_MCNT, W_ORDY, W_WCNT, W_PER = 16 };
static_assert(L_BYTES <= 160 * 1024 && L_BYTES > 80 * 1024, "one workgroup per CU");

struct Params { const u16* x[2]; const u16* k[2]; const u16* v[2]; const u16* wq[2]; const u16* wo[2]; u16* out; unsigned char* ws; };

__device__ __forceinline__ u64 rt() { return __builtin_amdgcn_s_memrealtime(); }
__device__ __forceinline__ unsigned rfl(unsigned v) { return (unsigned)__builtin_amdgcn_readfirstlane((int)v); }
__device__ __forceinline__ unsigned cvtpk(float lo, float hi) {
  unsigned r; asm("v_cvt_pk_bf16_f32 %0, %1, %2" : "=v"(r) : "v"(lo), "v"(hi)); return r;
}
__device__ __forceinline__ f32x4 mfma(v4u a, v4u b, f32x4 c) {
  return __builtin_amdgcn_mfma_f32_16x16x32_bf16(__builtin_bit_cast(bf16x8, a), __builtin_bit_cast(bf16x8, b), c, 0, 0, 0);
}
__device__ __forceinline__ float f(unsigned u) { return __uint_as_float(u); }
__device__ __forceinline__ unsigned u(float x) { return __float_as_uint(x); }
__device__ __forceinline__ float xrow_max(float x) {
  auto s = __builtin_amdgcn_permlane16_swap(u(x), u(x), false, false);
  x = fmaxf(f(s[0]), f(s[1]));
  auto t = __builtin_amdgcn_permlane32_swap(u(x), u(x), false, false);
  return fmaxf(f(t[0]), f(t[1]));
}
__device__ __forceinline__ float xrow_sum(float x) {
  auto s = __builtin_amdgcn_permlane16_swap(u(x), u(x), false, false);
  x = f(s[0]) + f(s[1]);
  auto t = __builtin_amdgcn_permlane32_swap(u(x), u(x), false, false);
  return f(t[0]) + f(t[1]);
}
template <int CTRL> __device__ __forceinline__ float dpp(float x) {
  return __builtin_bit_cast(float, __builtin_amdgcn_mov_dpp(__builtin_bit_cast(int, x), CTRL, 0xf, 0xf, true));
}
constexpr int ROR1 = 0x121, ROR2 = 0x122, ROR4 = 0x124, ROR8 = 0x128, BCAST = 0x150;
template <int N> struct IC { static constexpr int value = N; };
template <int I, int E, class F> __device__ __forceinline__ void each(F& fn) {
  if constexpr (I < E) { fn(IC<I>{}); each<I + 1, E>(fn); }
}
template <int N> __device__ __forceinline__ void vmwait() { asm volatile("s_waitcnt vmcnt(%0)" ::"i"(N) : "memory"); }
#define LDS_WAIT() asm volatile("s_waitcnt lgkmcnt(0)" ::: "memory")
#define LDS_ACQ() __builtin_amdgcn_fence(__ATOMIC_ACQUIRE, "workgroup")
#define SCHED() __builtin_amdgcn_sched_barrier(0)
#define PIN8(w) asm volatile("" : "+v"(w[0]), "+v"(w[1]), "+v"(w[2]), "+v"(w[3]), "+v"(w[4]), "+v"(w[5]), "+v"(w[6]), "+v"(w[7]) : : "memory")
#define GLDS(src, dst, imm) __builtin_amdgcn_global_load_lds((const GAS unsigned*)(src), (LAS unsigned*)(dst), 16, (imm), 2)

__device__ __forceinline__ v4u trfrag(const LAS unsigned char* lo, const LAS unsigned char* hi) {
  const v2u a = __builtin_bit_cast(v2u, __builtin_amdgcn_ds_read_tr16_b64_v4i16((LAS s16x4*)lo));
  const v2u b = __builtin_bit_cast(v2u, __builtin_amdgcn_ds_read_tr16_b64_v4i16((LAS s16x4*)hi));
  return v4u{a[0], a[1], b[0], b[1]};
}

template <int WX, int GAIN, int DEAD, int DH1, int STAMPS>
__global__ void __launch_bounds__(64 * NWAVE) k_fused(Params p) {
  constexpr int DQB = 600, QDELAY = 100, RT = 5, STAG = 0, WN = 16, HALF = 0, ALATE = 0;
  static_assert(WN % 4 == 0 && (HALF || WN % 8 == 0) && WN >= 8 && WN <= 48 && RT >= 0 && RT <= 5, "pieces left in flight; run of 2^RT tiles");
  extern __shared__ __attribute__((aligned(16))) unsigned char lds_raw[];
  LAS unsigned char* L = (LAS unsigned char*)lds_raw;
  volatile LAS unsigned* ctl = (volatile LAS unsigned*)(L + L_CTL);
  const u64 t_start = rt();
  const int bid = blockIdx.x, sidx = bid >> 5, part = bid & 31, b = sidx >> 2, kvh = sidx & 3;
  const int tid = threadIdx.x, lane = tid & 63, wave = (int)rfl(tid >> 6);
  const int c = lane & 15, g = lane >> 4;
  if (__builtin_expect(wave >= 1 && wave <= NCONS, 1)) {
    const unsigned char* wq = (const unsigned char*)p.wq[bid >> 7] + (size_t)(((bid >> 6) & 1) * 512 + (wave - 1) * 128) * 8192 + (bid & 63) * 128;
    const unsigned wq_off = (unsigned)(lane >> 3) * 8192u + (unsigned)(lane & 7) * 16u;
#pragma unroll
    for (int j = 0; j < 16; ++j) GLDS(wq + (size_t)j * 8 * 8192 + wq_off, L + L_RING + (wave - 1) * 2 * SLOT + j * 1024, 0);
  }
  u64* stamps = (u64*)(p.ws + WS_STAMP) + bid * 32;
  auto stamp = [&](int i) { if constexpr (STAMPS) { const u64 t = rt(); if (lane == 0) *(volatile LAS u64*)(L + L_BYTES + i * 8) = t; } };

  auto spin = [&](auto&& cond, auto nap) {
    for (unsigned it = 1; !cond(); ++it) {
      if constexpr (decltype(nap)::value > 0) __builtin_amdgcn_s_sleep(decltype(nap)::value);
      if ((it & 63u) == 0u) {
        if (rt() - t_start > TCAP) ctl[W_ABORT] = 1u;
        if (rfl(ctl[W_ABORT])) break;
      }
    }
  };
  auto wait_ge = [&](int w, unsigned n, auto nap) {
    spin([&] { return rfl(ctl[w]) >= n; }, nap);
    LDS_ACQ();
  };

  if (__builtin_expect(wave == 0, 1)) {
    __builtin_amdgcn_s_setprio(3);
    ctl[lane] = 0u; ctl[64 + lane] = 0u;
    unsigned koff[4], voff[2];
#pragma unroll
    for (int j = 0; j < 4; ++j) koff[j] = (unsigned)g * 256u + (unsigned)((c ^ (4 * j + g)) * 16);
#pragma unroll
    for (int j = 0; j < 2; ++j) voff[j] = (unsigned)g * 256u + (unsigned)((c ^ (2 * (4 * j + g))) * 16);
    constexpr int LAG = WN / 8 + 1;
    int fill = F_KA, lastpark = 0;
    u64 parked = 0;
    unsigned fr = 0u;
    auto full = [&](int fl) { ctl[W_FULL + (fl & (NSLOT - 1))] = (unsigned)(fl + 1); };
    auto drain = [&]() {
      vmwait<24>(); full(fill - 4); vmwait<16>(); full(fill - 3); vmwait<8>(); full(fill - 2); vmwait<0>(); full(fill - 1);
#pragma unroll
      for (int j = 5; j <= LAG + WX / 8; ++j) full(fill - j);
    };
    auto room = [&]() {
      if (rfl(fr) < (unsigned)(fill - NSLOT + 1) && fill >= NSLOT) {
        const u64 tp = rt();
        drain();
        spin([&] { return rfl(ctl[W_FREE + (fill & (NSLOT - 1))]) >= (unsigned)(fill - NSLOT + 1); }, IC<1>{});
        parked += rt() - tp; lastpark = fill;
      }
    };
    unsigned wd = 0u;
    int until = -1;
    auto issue = [&](const unsigned char* s0, const unsigned char* s1, unsigned o0, unsigned o1, unsigned o2, unsigned o3) {
      const unsigned dst = (unsigned)(size_t)(L + L_RING) + (unsigned)((fill & (NSLOT - 1)) * SLOT);
      room();
      if (WX > 0 && until < 0 && rfl(wd) != 0u) until = fill + (int)rfl(wd) - 1;
#define FILL(N) asm volatile("s_mov_b32 m0, %0\n\ts_waitcnt vmcnt(%7)\n\t"                                                     \
                   "global_load_lds_dwordx4 %3, %1 nt\n\tglobal_load_lds_dwordx4 %4, %1 offset:1024 nt\n\t"                    \
                   "global_load_lds_dwordx4 %5, %1 offset:2048 nt\n\tglobal_load_lds_dwordx4 %6, %1 offset:3072 nt\n\t"        \
                   "s_add_u32 m0, %0, 0x1000\n\ts_nop 0\n\t"                                                                  \
                   "global_load_lds_dwordx4 %3, %2 nt\n\tglobal_load_lds_dwordx4 %4, %2 offset:1024 nt\n\t"                    \
                   "global_load_lds_dwordx4 %5, %2 offset:2048 nt\n\tglobal_load_lds_dwordx4 %6, %2 offset:3072 nt"            \
                   :: "s"(dst), "s"(s0), "s"(s1), "v"(o0), "v"(o1), "v"(o2), "v"(o3), "i"(N) : "memory", "scc")
      if (WX > 0 && fill < until) {
        FILL(WN + WX);
        full(fill - LAG - WX / 8);
      } else {
        FILL(WN);
#pragma unroll
        for (int j = WX / 8; j >= 0; --j) full(fill - LAG - j);
      }
#undef FILL
      ++fill;
      fr = ctl[W_FREE + (fill & (NSLOT - 1))];
      if (WX > 0) wd = ctl[W_WIDE];
    };
    const size_t sbase = (size_t)sidx * SK * DH * 2;
    auto tile_off = [&](int i) -> size_t {
      constexpr int NR = NT >> RT;
      return (size_t)((((((i >> RT) + STAG * sidx) & (NR - 1)) * 32 + part) << RT) | (i & ((1 << RT) - 1))) * 8192;
    };
    const size_t wo_base = (size_t)((bid & 31) * 128) * 2048 + (bid >> 5) * 256;
    const unsigned woff = (unsigned)g * 2048u + (unsigned)c * 16u;
    LDS_WAIT();
    __builtin_amdgcn_s_barrier();
    stamp(11);
#pragma unroll 1
    for (int br = 0; br < 2; ++br) {
      const unsigned char* kb = (const unsigned char*)p.k[br] + sbase;
      const unsigned char* vb = (const unsigned char*)p.v[br] + sbase;
#pragma unroll 1
      for (int i = 0; i < NT; ++i) { const unsigned char* s = kb + tile_off(i); issue(s, s + 4096, koff[0], koff[1], koff[2], koff[3]); }
      stamp(24 + 2 * br);
#pragma unroll 1
      for (int i = 0; i < NT; ++i) { const unsigned char* s = vb + tile_off(i); issue(s, s + 4096, voff[0], voff[1], voff[0], voff[1]); }
      stamp(25 + 2 * br);
#pragma unroll 1
      for (int i = 0; i < 4; ++i) {
        const unsigned char* s = (const unsigned char*)p.wo[br] + wo_base + (size_t)(i * 32) * 2048;
        issue(s, s + 4 * 8192, woff, woff + 7168u, woff + 14336u, woff + 21504u);
      }
    }
    drain();
    stamp(9);
    if constexpr (STAMPS) { if (lane == 0) { *(volatile LAS u64*)(L + L_BYTES + 20 * 8) = parked; *(volatile LAS u64*)(L + L_BYTES + 21 * 8) = (u64)lastpark * 100; } }
    return;
  }

  const __amdgpu_buffer_rsrc_t rs = __builtin_amdgcn_make_buffer_rsrc(p.ws, 0, WS_END, 0x00020000);
  auto ld = [&](int off) -> v4u { return __builtin_amdgcn_raw_buffer_load_b128(rs, off, 0, 16); };
  auto st = [&](v4u v, int off) { __builtin_amdgcn_raw_buffer_store_b128(v, rs, off, 0, 16); };
  auto lds_add = [&](int w) { if (lane == 0) __hip_atomic_fetch_add((LAS unsigned*)&ctl[w], 1u, __ATOMIC_RELAXED, __HIP_MEMORY_SCOPE_WORKGROUP); };
  auto delay = [&](u64 ticks) { const u64 t0 = rt(); while (rt() - t0 < ticks) __builtin_amdgcn_s_sleep(2); };
  auto poll = [&](auto& v, auto& off, unsigned tA, unsigned tB, auto nap, auto&& shadow) {
    constexpr int N = sizeof(v) / sizeof(v[0]);
    spin([&] {
      v4u t[N];
#pragma unroll
      for (int j = 0; j < N; ++j) t[j] = ld(off[j]);
      SCHED();
      shadow();
      SCHED();
      bool pend = false;
#pragma unroll
      for (int j = 0; j < N; ++j) {
        const bool hit = (t[j][1] == tA) & (t[j][3] == tB) & (off[j] != OOB);
        v[j][0] = hit ? t[j][0] : v[j][0];
        v[j][1] = hit ? t[j][2] : v[j][1];
        off[j] = hit ? OOB : off[j];
        pend = pend | (off[j] != OOB);
      }
      return __any(pend) == 0;
    }, nap);
  };
  auto gather = [&](auto& v, int base, unsigned tA, unsigned tB, auto nap, auto&& shadow) {
    constexpr int N = sizeof(v) / sizeof(v[0]);
    int off[N];
#pragma unroll
    for (int j = 0; j < N; ++j) { off[j] = base + lane * 16 + 1024 * j; v[j] = v2u{0u, 0u}; }
    poll(v, off, tA, tB, nap, shadow);
  };
  const int tq = (lane >> 2) & 3, tp = lane & 3;
  const v4u zero4 = {0u, 0u, 0u, 0u};
  const f32x4 zf = {0.f, 0.f, 0.f, 0.f};
  auto pick = [&](f32x4 a0, f32x4 a1, f32x4 a2, f32x4 a3) -> f32x4 {
    const f32x4 lo = (c & 2) ? a1 : a0, hi = (c & 2) ? a3 : a2;
    return (c & 4) ? hi : lo;
  };
  LAS float* smax = (LAS float*)(L + L_STAT);
  LAS float* ssum = smax + 32;

  if (wave == NCONS + 1) {
    const u64 ep = __hip_atomic_load((GAS u64*)(p.ws + WS_EPOCH), __ATOMIC_RELAXED, __HIP_MEMORY_SCOPE_AGENT);
    __builtin_amdgcn_s_barrier();
    const unsigned tA = rfl((unsigned)ep) + 1u, tB = rfl((unsigned)(ep >> 32)) + 1u;
    ctl[W_TAGA] = tA; ctl[W_TAGB] = tB;
    LDS_WAIT();
    ctl[W_TAGOK] = 1u;
    auto reduce = [&](auto nap) {
      v2u r[4];
      gather(r, WS_H2 + ((bid & 1) * 128 + (bid >> 1)) * 4096, tA, tB, nap, [] {});
      float s0 = (f(r[0][0]) + f(r[1][0])) + (f(r[2][0]) + f(r[3][0])), s1 = (f(r[0][1]) + f(r[1][1])) + (f(r[2][1]) + f(r[3][1]));
      s0 += dpp<ROR8>(s0); s1 += dpp<ROR8>(s1);
      s0 = xrow_sum(s0); s1 = xrow_sum(s1);
      if (lane < 8) {
        const int col = 32 * (bid >> 3) + 16 * ((lane >> 1) & 1) + 4 * ((bid >> 1) & 3) + 2 * (lane >> 2);
        *(GAS unsigned*)(p.out + (lane & 1) * 2048 + (bid & 1) * 1024 + col) = cvtpk(s0, s1);
      }
    };
    wait_ge(W_ORDY + W_PER, (unsigned)NCONS, IC<2>{});
    stamp(17);
    reduce(IC<0>{});
    if (bid == 1 && lane == 0)
      __hip_atomic_store((GAS u64*)(p.ws + WS_EPOCH), ((u64)tB << 32) | tA, __ATOMIC_RELAXED, __HIP_MEMORY_SCOPE_AGENT);
    if constexpr (STAMPS) {
      stamp(18);
      delay(300);
      if (lane < 32) stamps[lane] = lane == 0 ? t_start : *(volatile LAS u64*)(L + L_BYTES + lane * 8);
    }
    return;
  }

  if (wave > NCONS + 1) {
    const int tw = wave - NCONS - 2;
    __builtin_amdgcn_s_barrier();
    wait_ge(W_QPUB, 1u, IC<2>{});
    const unsigned tA = rfl(ctl[W_TAGA]), tB = rfl(ctl[W_TAGB]);
#pragma unroll 1
    for (int br = 0; br < 2; ++br) {
      delay(br ? DQB : QDELAY);
      v2u q[4];
      int off[4];
      const int qb = WS_Q + ((br * 4 + kvh) * 2 + b) * 16384 + tw * 2048 + lane * 16;
#pragma unroll
      for (int j = 0; j < 4; ++j) { off[j] = qb + (j >> 1) * 8192 + (j & 1) * 1024; q[j] = v2u{0u, 0u}; }
      poll(q, off, tA, tB, IC<4>{}, [] {});
      LAS unsigned* qi = (LAS unsigned*)(L + L_QIMG + br * 2048 + tw * 512 + lane * 4);
      qi[0] = cvtpk(f(q[0][0]) + f(q[2][0]), f(q[0][1]) + f(q[2][1]));
      qi[64] = cvtpk(f(q[1][0]) + f(q[3][0]), f(q[1][1]) + f(q[3][1]));
      LDS_WAIT();
      lds_add(W_QRDY + W_PER * br);
      if (tw == 0) stamp(15 + br);
    }
    v4u held = zero4;
#pragma unroll 1
    for (int br = 0; br < 2; ++br) {
      const int wb = W_PER * br, f0 = br ? F_WOB : F_WOA;
      const int o2 = c < 8 ? WS_H2 + (br * 128 + ((bid >> 5) * 4 + tw) * 4 + g) * 4096 + (bid & 31) * 128 + c * 16 : OOB;
      v4u wf[8];
      bool have_wo = false;
      auto try_wo = [&]() -> bool {
        if (rfl(ctl[W_FULL + ((f0 + 3) & (NSLOT - 1))]) < (unsigned)(f0 + 4)) return false;
        LDS_ACQ();
        const LAS unsigned char* s = L + L_RING + (f0 & (NSLOT - 1)) * SLOT + (8 * g + tq) * 256 + tw * 64 + tp * 8;
#pragma unroll
        for (int j = 0; j < 8; ++j) wf[j] = trfrag(s + (j >> 1) * 8192 + (j & 1) * 32, s + (j >> 1) * 8192 + (j & 1) * 32 + 1024);
        LDS_WAIT();
        PIN8(wf);
        unsigned old = 0u;
        if (lane == 0) old = __hip_atomic_fetch_add((LAS unsigned*)&ctl[W_WCNT + wb], 1u, __ATOMIC_RELAXED, __HIP_MEMORY_SCOPE_WORKGROUP);
        if (rfl(old) == 3u && lane < 4) ctl[W_FREE + ((f0 + lane) & (NSLOT - 1))] = (unsigned)(f0 + lane + 1);
        have_wo = true;
        return true;
      };
      v2u d[9];
      int off[9];
#pragma unroll
      for (int j = 0; j < 8; ++j) { off[j] = WS_H1 + ((br * 32 + (bid & 31)) * 4 + tw) * 8192 + lane * 16 + 1024 * j; d[j] = v2u{0u, 0u}; }
      off[8] = WS_ML + (br * 8 + (tw >> 1) * 4 + ((bid >> 3) & 3)) * 4096 + (4 * (c & 7) + g) * 128 + (bid & 7) * 16;
      d[8] = v2u{0u, 0u};
      if (br == 0) {
        spin(try_wo, IC<16>{});
        wait_ge(W_MCNT + (ALATE ? W_PER : 0), (unsigned)(ALATE ? 1 : NCONS), IC<8>{});
        if (!ALATE) delay(DH1);
        poll(d, off, tA, tB, IC<(ALATE ? 0 : 16)>{}, [] {});
      } else {
        wait_ge(W_MCNT + wb, (unsigned)NCONS, IC<2>{});
        if (!ALATE) st(held, o2 - 128 * 4096);
        poll(d, off, tA, tB, IC<0>{}, [&] { if (!have_wo) try_wo(); });
      }
      if (WX > 0 && br == 0 && tw == 0) {
        float lag = (float)(int)(rfl(ctl[W_STAMP]) - d[8][1]);
        lag += dpp<ROR1>(lag); lag += dpp<ROR2>(lag); lag += dpp<ROR4>(lag);
        const int ticks = (int)f(rfl(u(xrow_sum(lag)))) / 32;
        const int n = ticks > DEAD ? min(ticks * GAIN / 100, 64) : 0;
        ctl[W_WIDE] = (unsigned)(n + 1);
        if constexpr (STAMPS) { if (lane == 0) *(volatile LAS u64*)(L + L_BYTES + 23 * 8) = (u64)n * 100; }
      }
      float M = f(d[8][0]);
      M = fmaxf(M, dpp<ROR1>(M)); M = fmaxf(M, dpp<ROR2>(M)); M = fmaxf(M, dpp<ROR4>(M));
      M = xrow_max(M);
      const float w = __builtin_amdgcn_exp2f(f(d[8][0]) - M);
      float S = w;
      S += dpp<ROR1>(S); S += dpp<ROR2>(S); S += dpp<ROR4>(S);
      const float inv = 1.0f / xrow_sum(S);
      f32x4 o = zf;
#define ACC(J) o += f32x4{f(d[J][0] << 16), f(d[J][0] & 0xffff0000u), f(d[J][1] << 16), f(d[J][1] & 0xffff0000u)} * dpp<BCAST + J>(w);
      ACC(0) ACC(1) ACC(2) ACC(3) ACC(4) ACC(5) ACC(6) ACC(7)
#undef ACC
      const v2u pk = {cvtpk(xrow_sum(o[0]) * inv, xrow_sum(o[1]) * inv), cvtpk(xrow_sum(o[2]) * inv, xrow_sum(o[3]) * inv)};
      if (lane < 16) *(LAS v2u*)(L + L_OIMG + br * 512 + tw * 128 + lane * 8) = pk;
      LDS_WAIT();
      lds_add(W_ORDY + wb);
      if (tw == 0 && br == 1) stamp(1);
      if (!have_wo) spin(try_wo, IC<0>{});
      wait_ge(W_ORDY + wb, (unsigned)NCONS, IC<0>{});
      f32x4 a0 = zf, a1 = zf;
#pragma unroll
      for (int ks = 0; ks < 4; ++ks) {
        const v4u of = *(const LAS v4u*)(L + L_OIMG + br * 512 + (c & 1) * 256 + ks * 64 + g * 16);
        a0 = mfma(wf[2 * ks], of, a0);
        a1 = mfma(wf[2 * ks + 1], of, a1);
      }
      const f32x4 r = (c & 2) ? a1 : a0;
      held = v4u{u((c & 4) ? r[2] : r[0]), tA, u((c & 4) ? r[3] : r[1]), tB};
      if (br == 1 || ALATE) st(held, o2);
      if (tw == 0) stamp(br ? 7 : 22);
    }
    return;
  }

  const int ci = wave - 1;
  v4u xf[4];
#pragma unroll
  for (int ks = 0; ks < 4; ++ks) xf[ks] = *(const GAS v4u*)(p.x[bid >> 7] + (c & 1) * 1024 + ((bid >> 6) & 1) * 512 + ci * 128 + ks * 32 + 8 * g);
  __builtin_amdgcn_s_barrier();
  vmwait<0>();
  {
    const LAS unsigned char* s = L + L_RING + ci * 2 * SLOT + (8 * g + tq) * 128 + tp * 8;
    v4u w0[8], w1[8];
#pragma unroll
    for (int j = 0; j < 8; ++j) {
      const int o = (j >> 2) * 4096 + (j & 3) * 32;
      w0[j] = trfrag(s + o, s + o + 512); w1[j] = trfrag(s + 8192 + o, s + 8192 + o + 512);
    }
    LDS_WAIT();
    if (lane < 2) ctl[W_FREE + 2 * ci + lane] = (unsigned)(2 * ci + lane + 1);
    PIN8(w0); PIN8(w1);
    f32x4 a[4];
#pragma unroll
    for (int e = 0; e < 4; ++e) a[e] = mfma(w1[4 + e], xf[3], mfma(w1[e], xf[2], mfma(w0[4 + e], xf[1], mfma(w0[e], xf[0], zf))));
    const f32x4 r = pick(a[0], a[1], a[2], a[3]);
    if (c < 8) *(LAS f32x4*)(L + L_QRED + ci * 512 + (c & 1) * 256 + (c >> 1) * 64 + g * 16) = r;
    LDS_WAIT();
    lds_add(W_QCNT);
  }
  wait_ge(W_TAGOK, 1u, IC<0>{});
  const unsigned tA = rfl(ctl[W_TAGA]), tB = rfl(ctl[W_TAGB]);
  if (ci == 0) {
    wait_ge(W_QCNT, (unsigned)NCONS, IC<0>{});
    f32x2 q = {0.f, 0.f};
#pragma unroll
    for (int w = 0; w < NCONS; ++w) q += *(const LAS f32x2*)(L + L_QRED + w * 512 + lane * 8);
    st(v4u{u(q[0]), tA, u(q[1]), tB},
       WS_Q + ((((bid >> 7) * 4 + ((bid >> 4) & 3)) * 2 + (lane >> 5)) * 2 + ((bid >> 6) & 1)) * 8192 + (bid & 15) * 512 + (lane & 31) * 16);
    ctl[W_QPUB] = 1u;
  }
  if (ci == 0) stamp(3);

  v4u keep = zero4, keepl = zero4;
#pragma unroll 1
  for (int br = 0; br < 2; ++br) {
    const int wb = W_PER * br, fk = (br ? F_KB : F_KA) + ci, fv = (br ? F_VB : F_VA) + ci;
    f32x4 sc[TPC][2];
    v4u qf[4];
    unsigned kr = (unsigned)c * 256u + (unsigned)((g ^ c) * 16);
    asm volatile("" : "+v"(kr));
    auto ktile = [&](auto tc) {
      constexpr int t = decltype(tc)::value;
      const int fl = fk + NCONS * t, slot = fl & (NSLOT - 1);
      wait_ge(W_FULL + slot, (unsigned)(fl + 1), IC<1>{});
      v4u K[8];
#pragma unroll
      for (int j = 0; j < 8; ++j) K[j] = *(const LAS v4u*)(L + L_RING + ((kr + slot * SLOT) ^ (64u * (j & 3))) + (j >> 2) * 4096);
      LDS_WAIT();
      ctl[W_FREE + slot] = (unsigned)(fl + 1);
      PIN8(K);
      if constexpr (t == 0) {
        wait_ge(W_QRDY + wb, (unsigned)NCONS, IC<1>{});
#pragma unroll
        for (int j = 0; j < 4; ++j) {
          const v4u q = *(const LAS v4u*)(L + L_QIMG + br * 2048 + (c & 7) * 256 + j * 64 + g * 16);
          qf[j] = c < 8 ? q : zero4;
        }
        if (ci == 0 && br == 0) stamp(4);
      }
      f32x4 s0 = zf, s1 = zf;
#pragma unroll
      for (int j = 0; j < 4; ++j) { s0 = mfma(K[j], qf[j], s0); s1 = mfma(K[4 + j], qf[j], s1); }
      sc[t][0] = s0; sc[t][1] = s1;
    };
    each<0, TPC>(ktile);
    {
      float m = -INFINITY;
#pragma unroll
      for (int t = 0; t < TPC; ++t)
#pragma unroll
        for (int j = 0; j < 2; ++j) m = fmaxf(m, fmaxf(fmaxf(sc[t][j][0], sc[t][j][1]), fmaxf(sc[t][j][2], sc[t][j][3])));
      m = xrow_max(m) * C2;
      if (lane < 8) smax[ci * 8 + lane] = m;
      LDS_WAIT();
      lds_add(W_XCNT + wb);
    }
    wait_ge(W_XCNT + wb, (unsigned)NCONS, IC<0>{});
    v4u pf[TPC];
    {
      const int h = c & 7;
      const float m = fmaxf(fmaxf(smax[h], smax[8 + h]), fmaxf(smax[16 + h], smax[24 + h]));
      float l = 0.f;
#pragma unroll
      for (int t = 0; t < TPC; ++t) {
        float pr[8];
#pragma unroll
        for (int i = 0; i < 8; ++i) pr[i] = __builtin_amdgcn_exp2f(fmaf(sc[t][i >> 2][i & 3], C2, -m));
        pf[t] = v4u{cvtpk(pr[0], pr[1]), cvtpk(pr[2], pr[3]), cvtpk(pr[4], pr[5]), cvtpk(pr[6], pr[7])};
        l += ((pr[0] + pr[1]) + (pr[2] + pr[3])) + ((pr[4] + pr[5]) + (pr[6] + pr[7]));
      }
      l = xrow_sum(l);
      if (lane < 8) ssum[ci * 8 + lane] = l;
      LDS_WAIT();
      lds_add(W_LCNT + wb);
    }
    const int h = 2 * ci + (lane >> 5), l5 = lane & 31;
    wait_ge(W_LCNT + wb, (unsigned)NCONS, IC<0>{});
    const float inv = 1.0f / ((ssum[h] + ssum[8 + h]) + (ssum[16 + h] + ssum[24 + h]));
    const int h8 = lane & 7;
    const float lse = fmaxf(fmaxf(smax[h8], smax[8 + h8]), fmaxf(smax[16 + h8], smax[24 + h8]))
                    + __builtin_amdgcn_logf((ssum[h8] + ssum[8 + h8]) + (ssum[16 + h8] + ssum[24 + h8]));
    f32x4 acc[8];
#pragma unroll
    for (int e = 0; e < 8; ++e) acc[e] = zf;
    unsigned vr = (unsigned)(4 * g + tq) * 256u + (unsigned)((4 * (g & 1) + tq) * 32 + tp * 8);
    asm volatile("" : "+v"(vr));
    auto vtile = [&](auto tc) {
      constexpr int t = decltype(tc)::value;
      const int fl = fv + NCONS * t, slot = fl & (NSLOT - 1);
      wait_ge(W_FULL + slot, (unsigned)(fl + 1), IC<1>{});
      if (t == TPC - 1 && ci == 3 && br == 1) stamp(19);
      v4u V[8];
#pragma unroll
      for (int e = 0; e < 8; ++e) { const LAS unsigned char* s = L + L_RING + ((vr + slot * SLOT) ^ (32u * e)); V[e] = trfrag(s, s + 4096); }
      LDS_WAIT();
      ctl[W_FREE + slot] = (unsigned)(fl + 1);
      PIN8(V);
#pragma unroll
      for (int e = 0; e < 8; ++e) acc[e] = mfma(V[e], pf[t], acc[e]);
    };
    each<0, TPC>(vtile);
    if (c < 8) {
      LAS unsigned char* row = L + L_MRG + (ci * 8 + c) * MROW + g * 16;
#pragma unroll
      for (int e = 0; e < 8; ++e) *(LAS f32x4*)(row + e * 64) = acc[e];
    }
    LDS_WAIT();
    lds_add(W_MCNT + wb);
    const int rec = WS_H1 + ((br * 32 + kvh * 8 + h) * 4 + b * 2 + (l5 >> 4)) * 8192 + part * 256 + (l5 & 15) * 16;
    const int mlo = lane < 8 && ci == 0 ? WS_ML + (br * 8 + sidx) * 4096 + part * 128 + lane * 16 : OOB;
    if (ALATE && br == 1) { st(keep, rec - 32 * 4 * 8192); st(keepl, mlo == OOB ? OOB : mlo - 8 * 4096); }
    wait_ge(W_MCNT + wb, (unsigned)NCONS, IC<0>{});
    const LAS unsigned char* mr = L + L_MRG + h * MROW + l5 * 16;
    const f32x4 o = ((*(const LAS f32x4*)mr + *(const LAS f32x4*)(mr + 8 * MROW)) + (*(const LAS f32x4*)(mr + 16 * MROW) + *(const LAS f32x4*)(mr + 24 * MROW))) * inv;
    keep = v4u{cvtpk(o[0], o[1]), tA, cvtpk(o[2], o[3]), tB};
    keepl = v4u{u(lse), tA, (unsigned)rt(), tB};
    if (ci == 0 && br == 0) ctl[W_STAMP] = rfl(keepl[2]);
    if (!ALATE || br == 1) { st(keep, rec); st(keepl, mlo); }
    if (ci == 3) stamp(br ? 6 : 5);
  }
}

template <int WX, int GAIN, int DEAD, int DH1, int STAMPS>
static void launch(void* const* d_in, void* d_out, void* d_ws, hipStream_t stream) {
  Params p;
  for (int br = 0; br < 2; ++br) {
    p.x[br] = (const u16*)d_in[5 * br + 0]; p.k[br] = (const u16*)d_in[5 * br + 1]; p.v[br] = (const u16*)d_in[5 * br + 2];
    p.wq[br] = (const u16*)d_in[5 * br + 3]; p.wo[br] = (const u16*)d_in[5 * br + 4];
  }
  p.out = (u16*)d_out; p.ws = (unsigned char*)d_ws;
  constexpr int bytes = L_BYTES + (STAMPS ? 256 : 0);
  (void)hipFuncSetAttribute((const void*)k_fused<WX, GAIN, DEAD, DH1, STAMPS>, hipFuncAttributeMaxDynamicSharedMemorySize, bytes);
  hipLaunchKernelGGL((k_fused<WX, GAIN, DEAD, DH1, STAMPS>), dim3(NWG), dim3(64 * NWAVE), bytes, stream, p);
}

}

extern "C" void kernel_launch(void* const* d_in, const int* in_sizes, int n_in, void* d_out, int out_size, void* d_ws, size_t ws_size,
                              hipStream_t stream) {
  fused::launch<CFG>(d_in, d_out, (char*)d_ws + ((ws_size - (8u << 20)) & ~(size_t)0x1fffff), stream);
}
```

```cpp
#include <hip/hip_runtime.h>
#include <hip/hip_bf16.h>
#include <cstdint>

#define GAS __attribute__((address_space(1)))
#define LAS __attribute__((address_space(3)))
typedef unsigned v4u __attribute__((ext_vector_type(4)));
typedef unsigned v2u __attribute__((ext_vector_type(2)));
typedef float f32x4 __attribute__((ext_vector_type(4)));
typedef float f32x2 __attribute__((ext_vector_type(2)));
typedef short bf16x8 __attribute__((ext_vector_type(8)));
typedef short s16x4 __attribute__((ext_vector_type(4)));
typedef unsigned short u16;
typedef unsigned long long u64;

#define CFG 80, 250, 20, 30, 16, 0

namespace fused {

constexpr int SK = 32768, DH = 128;
constexpr int NWG = 256, NCONS = 4, NWAVE = 10;
constexpr int NSLOT = 16, SLOT = 8192;
constexpr int NT = 32, TPC = NT / NCONS;
constexpr int F_KA = 8, F_VA = F_KA + NT, F_WOA = F_VA + NT, F_KB = F_WOA + 4, F_VB = F_KB + NT, F_WOB = F_VB + NT;
constexpr float C2 = 0.088388347648318440f * 1.4426950408889634f;
constexpr u64 TCAP = 2000000;
constexpr int OOB = 0x7ffffff0;

constexpr int WS_EPOCH = 0;
constexpr int WS_ML = 4096;
constexpr int WS_Q = WS_ML + 2 * 8 * 4096;
constexpr int WS_H2 = WS_Q + 2 * 8 * 16384;
constexpr int WS_H1 = WS_H2 + 256 * 4096;
constexpr int WS_END = WS_H1 + 2 * 128 * 8192;
constexpr int WS_STAMP = WS_END;

constexpr int L_RING = 0;
constexpr int L_CTL = NSLOT * SLOT;
constexpr int L_QIMG = L_CTL + 512;
constexpr int L_QRED = L_QIMG + 4096;
constexpr int L_OIMG = L_QRED + 2048;
constexpr int L_STAT = L_OIMG + 1024;
constexpr int MROW = 528;
constexpr int L_MRG = L_STAT + 512;
constexpr int L_BYTES = L_MRG + 4 * 8 * MROW;
enum { W_FULL = 0, W_FREE = 16, W_ABORT = 32, W_TAGA, W_TAGB, W_TAGOK, W_STAMP, W_HOLD, W_BASE, W_TAU,
       W_QCNT = 40, W_QPUB, W_QRDY, W_XCNT, W_LCNT, W_SRDY, W_MCNT, W_ORDY, W_WCNT, W_PER = 16 };
static_assert(L_BYTES <= 160 * 1024 && L_BYTES > 80 * 1024, "one workgroup per CU");

struct Params { const u16* x[2]; const u16* k[2]; const u16* v[2]; const u16* wq[2]; const u16* wo[2]; u16* out; unsigned char* ws; };

__device__ __forceinline__ u64 rt() { return __builtin_amdgcn_s_memrealtime(); }
__device__ __forceinline__ unsigned rfl(unsigned v) { return (unsigned)__builtin_amdgcn_readfirstlane((int)v); }
__device__ __forceinline__ unsigned cvtpk(float lo, float hi) {
  unsigned r; asm("v_cvt_pk_bf16_f32 %0, %1, %2" : "=v"(r) : "v"(lo), "v"(hi)); return r;
}
__device__ __forceinline__ f32x4 mfma(v4u a, v4u b, f32x4 c) {
  return __builtin_amdgcn_mfma_f32_16x16x32_bf16(__builtin_bit_cast(bf16x8, a), __builtin_bit_cast(bf16x8, b), c, 0, 0, 0);
}
__device__ __forceinline__ float f(unsigned u) { return __uint_as_float(u); }
__device__ __forceinline__ unsigned u(float x) { return __float_as_uint(x); }
__device__ __forceinline__ float xrow_max(float x) {
  auto s = __builtin_amdgcn_permlane16_swap(u(x), u(x), false, false);
  x = fmaxf(f(s[0]), f(s[1]));
  auto t = __builtin_amdgcn_permlane32_swap(u(x), u(x), false, false);
  return fmaxf(f(t[0]), f(t[1]));
}
__device__ __forceinline__ float xrow_sum(float x) {
  auto s = __builtin_amdgcn_permlane16_swap(u(x), u(x), false, false);
  x = f(s[0]) + f(s[1]);
  auto t = __builtin_amdgcn_permlane32_swap(u(x), u(x), false, false);
  return f(t[0]) + f(t[1]);
}
template <int CTRL> __device__ __forceinline__ float dpp(float x) {
  return __builtin_bit_cast(float, __builtin_amdgcn_mov_dpp(__builtin_bit_cast(int, x), CTRL, 0xf, 0xf, true));
}
constexpr int ROR1 = 0x121, ROR2 = 0x122, ROR4 = 0x124, ROR8 = 0x128, BCAST = 0x150;
template <int N> struct IC { static constexpr int value = N; };
template <int I, int E, class F> __device__ __forceinline__ void each(F& fn) {
  if constexpr (I < E) { fn(IC<I>{}); each<I + 1, E>(fn); }
}
template <int N> __device__ __forceinline__ void vmwait() { asm volatile("s_waitcnt vmcnt(%0)" ::"i"(N) : "memory"); }
#define LDS_WAIT() asm volatile("s_waitcnt lgkmcnt(0)" ::: "memory")
#define LDS_ACQ() __builtin_amdgcn_fence(__ATOMIC_ACQUIRE, "workgroup")
#define SCHED() __builtin_amdgcn_sched_barrier(0)
#define PIN8(w) asm volatile("" : "+v"(w[0]), "+v"(w[1]), "+v"(w[2]), "+v"(w[3]), "+v"(w[4]), "+v"(w[5]), "+v"(w[6]), "+v"(w[7]) : : "memory")
#define GLDS(src, dst, imm) __builtin_amdgcn_global_load_lds((const GAS unsigned*)(src), (LAS unsigned*)(dst), 16, (imm), 2)

__device__ __forceinline__ v4u trfrag(const LAS unsigned char* lo, const LAS unsigned char* hi) {
  const v2u a = __builtin_bit_cast(v2u, __builtin_amdgcn_ds_read_tr16_b64_v4i16((LAS s16x4*)lo));
  const v2u b = __builtin_bit_cast(v2u, __builtin_amdgcn_ds_read_tr16_b64_v4i16((LAS s16x4*)hi));
  return v4u{a[0], a[1], b[0], b[1]};
}

template <int SOFF, int HOFF, int BEH, int LEAD, int WX, int STAMPS>
__global__ void __launch_bounds__(64 * NWAVE) k_fused(Params p) {
  constexpr int DQB = 600, QDELAY = 100, RT = 5, STAG = 0, WN = 16, HALF = 0, ALATE = 0, TPM = 1000, DH1 = 250, NARROW = 0, D1 = 70, D2 = 60, HELD = 0;
  static_assert(WN % 4 == 0 && (HALF || WN % 8 == 0) && WN >= 8 && WN <= 48 && RT >= 0 && RT <= 5, "pieces left in flight; run of 2^RT tiles");
  extern __shared__ __attribute__((aligned(16))) unsigned char lds_raw[];
  LAS unsigned char* L = (LAS unsigned char*)lds_raw;
  volatile LAS unsigned* ctl = (volatile LAS unsigned*)(L + L_CTL);
  const u64 t_start = rt();
  const int bid = blockIdx.x, sidx = bid >> 5, part = bid & 31, b = sidx >> 2, kvh = sidx & 3;
  const int tid = threadIdx.x, lane = tid & 63, wave = (int)rfl(tid >> 6);
  const int c = lane & 15, g = lane >> 4;
  if (__builtin_expect(wave >= 1 && wave <= NCONS, 1)) {
    const unsigned char* wq = (const unsigned char*)p.wq[bid >> 7] + (size_t)(((bid >> 6) & 1) * 512 + (wave - 1) * 128) * 8192 + (bid & 63) * 128;
    const unsigned wq_off = (unsigned)(lane >> 3) * 8192u + (unsigned)(lane & 7) * 16u;
#pragma unroll
    for (int j = 0; j < 16; ++j) GLDS(wq + (size_t)j * 8 * 8192 + wq_off, L + L_RING + (wave - 1) * 2 * SLOT + j * 1024, 0);
  }
  u64* stamps = (u64*)(p.ws + WS_STAMP) + bid * 32;
  auto stamp = [&](int i) { if constexpr (STAMPS) { const u64 t = rt(); if (lane == 0) *(volatile LAS u64*)(L + L_BYTES + i * 8) = t; } };

  auto spin = [&](auto&& cond, auto nap) {
    for (unsigned it = 1; !cond(); ++it) {
      if constexpr (decltype(nap)::value > 0) __builtin_amdgcn_s_sleep(decltype(nap)::value);
      if ((it & 63u) == 0u) {
        if (rt() - t_start > TCAP) ctl[W_ABORT] = 1u;
        if (rfl(ctl[W_ABORT])) break;
      }
    }
  };
  auto wait_ge = [&](int w, unsigned n, auto nap) {
    spin([&] { return rfl(ctl[w]) >= n; }, nap);
    LDS_ACQ();
  };

  if (__builtin_expect(wave == 0, 1)) {
    __builtin_amdgcn_s_setprio(3);
    ctl[lane] = 0u; ctl[64 + lane] = 0u;
    unsigned koff[4], voff[2];
#pragma unroll
    for (int j = 0; j < 4; ++j) koff[j] = (unsigned)g * 256u + (unsigned)((c ^ (4 * j + g)) * 16);
#pragma unroll
    for (int j = 0; j < 2; ++j) voff[j] = (unsigned)g * 256u + (unsigned)((c ^ (2 * (4 * j + g))) * 16);
    constexpr int LAG = WN / 8 + 1;
    int fill = F_KA, lastpark = 0;
    u64 parked = 0;
    unsigned fr = 0u;
    auto full = [&](int fl) { ctl[W_FULL + (fl & (NSLOT - 1))] = (unsigned)(fl + 1); };
    auto drain = [&]() {
      static_assert(LAG + WX / 8 <= 6, "");
      vmwait<40>(); full(fill - 6); vmwait<32>(); full(fill - 5); vmwait<24>(); full(fill - 4);
      vmwait<16>(); full(fill - 3); vmwait<8>(); full(fill - 2); vmwait<0>(); full(fill - 1);
    };
    auto room = [&]() {
      if (rfl(fr) < (unsigned)(fill - NSLOT + 1) && fill >= NSLOT) {
        const u64 tp = rt();
        drain();
        spin([&] { return rfl(ctl[W_FREE + (fill & (NSLOT - 1))]) >= (unsigned)(fill - NSLOT + 1); }, IC<1>{});
        parked += rt() - tp; lastpark = fill;
      }
    };
    v2u sched = {0u, 0u};
    unsigned now = 0u, nwide = 0u;
    auto issue = [&](const unsigned char* s0, const unsigned char* s1, unsigned o0, unsigned o1, unsigned o2, unsigned o3) {
      const unsigned dst = (unsigned)(size_t)(L + L_RING) + (unsigned)((fill & (NSLOT - 1)) * SLOT);
      room();
      bool wide = false;
      const unsigned tau = rfl(sched[1]);
      if (tau != 0u) {
        const unsigned due = rfl(sched[0]) + ((unsigned)fill * tau >> 8);
        int late = (int)(now - due);
        wide = late > BEH && fill < F_WOB - NARROW;
        while (late < -LEAD && late > -1000) { __builtin_amdgcn_s_sleep(1); late = (int)((unsigned)rt() - due); }
      }
#define FILL(N) asm volatile("s_mov_b32 m0, %0\n\ts_waitcnt vmcnt(%7)\n\t"                                                     \
                   "global_load_lds_dwordx4 %3, %1 nt\n\tglobal_load_lds_dwordx4 %4, %1 offset:1024 nt\n\t"                    \
                   "global_load_lds_dwordx4 %5, %1 offset:2048 nt\n\tglobal_load_lds_dwordx4 %6, %1 offset:3072 nt\n\t"        \
                   "s_add_u32 m0, %0, 0x1000\n\ts_nop 0\n\t"                                                                  \
                   "global_load_lds_dwordx4 %3, %2 nt\n\tglobal_load_lds_dwordx4 %4, %2 offset:1024 nt\n\t"                    \
                   "global_load_lds_dwordx4 %5, %2 offset:2048 nt\n\tglobal_load_lds_dwordx4 %6, %2 offset:3072 nt"            \
                   :: "s"(dst), "s"(s0), "s"(s1), "v"(o0), "v"(o1), "v"(o2), "v"(o3), "i"(N) : "memory", "scc")
      if (wide) {
        ++nwide;
        FILL(WN + WX);
        full(fill - LAG - WX / 8);
      } else {
        FILL(WN);
#pragma unroll
        for (int j = WX / 8; j >= 0; --j) full(fill - LAG - j);
      }
#undef FILL
      ++fill;
      fr = ctl[W_FREE + (fill & (NSLOT - 1))];
      sched = *(volatile LAS v2u*)&ctl[W_BASE];
      now = (unsigned)rt();
    };
    const size_t sbase = (size_t)sidx * SK * DH * 2;
    auto tile_off = [&](int i) -> size_t {
      constexpr int NR = NT >> RT;
      return (size_t)((((((i >> RT) + STAG * sidx) & (NR - 1)) * 32 + part) << RT) | (i & ((1 << RT) - 1))) * 8192;
    };
    const size_t wo_base = (size_t)((bid & 31) * 128) * 2048 + (bid >> 5) * 256;
    const unsigned woff = (unsigned)g * 2048u + (unsigned)c * 16u;
    LDS_WAIT();
    __builtin_amdgcn_s_barrier();
    stamp(11);
#pragma unroll 1
    for (int br = 0; br < 2; ++br) {
      const unsigned char* kb = (const unsigned char*)p.k[br] + sbase;
      const unsigned char* vb = (const unsigned char*)p.v[br] + sbase;
#pragma unroll 1
      for (int i = 0; i < NT; ++i) { const unsigned char* s = kb + tile_off(i); issue(s, s + 4096, koff[0], koff[1], koff[2], koff[3]); }
      stamp(24 + 2 * br);
      const unsigned tk = (unsigned)rt();
#pragma unroll 1
      for (int i = 0; i < NT; ++i) { const unsigned char* s = vb + tile_off(i); issue(s, s + 4096, voff[0], voff[1], voff[0], voff[1]); }
      stamp(25 + 2 * br);
      if (br == 0) { const unsigned tv = (unsigned)rt(); ctl[W_STAMP] = (tv << 12) | ((tv - tk) & 0xfffu); }
#pragma unroll 1
      for (int i = 0; i < 4; ++i) {
        const unsigned char* s = (const unsigned char*)p.wo[br] + wo_base + (size_t)(i * 32) * 2048;
        issue(s, s + 4 * 8192, woff, woff + 7168u, woff + 14336u, woff + 21504u);
      }
    }
    drain();
    stamp(9);
    if constexpr (STAMPS) { if (lane == 0) *(volatile LAS u64*)(L + L_BYTES + 23 * 8) = (u64)nwide * 100; }
    if constexpr (STAMPS) { if (lane == 0) { *(volatile LAS u64*)(L + L_BYTES + 20 * 8) = parked; *(volatile LAS u64*)(L + L_BYTES + 21 * 8) = (u64)lastpark * 100; } }
    return;
  }

  const __amdgpu_buffer_rsrc_t rs = __builtin_amdgcn_make_buffer_rsrc(p.ws, 0, WS_END, 0x00020000);
  auto ld = [&](int off) -> v4u { return __builtin_amdgcn_raw_buffer_load_b128(rs, off, 0, 16); };
  auto st = [&](v4u v, int off) { __builtin_amdgcn_raw_buffer_store_b128(v, rs, off, 0, 16); };
  auto lds_add = [&](int w) { if (lane == 0) __hip_atomic_fetch_add((LAS unsigned*)&ctl[w], 1u, __ATOMIC_RELAXED, __HIP_MEMORY_SCOPE_WORKGROUP); };
  auto delay = [&](u64 ticks) { const u64 t0 = rt(); while (rt() - t0 < ticks) __builtin_amdgcn_s_sleep(2); };
  auto hold = [&](int extra) {
    const unsigned h = rfl(ctl[W_HOLD]);
    if (HOFF > -1000 && h != 0u) {
      for (;;) {
        const int left = ((int)(h - ((unsigned)rt() << 12)) >> 12) + extra;
        if (left <= 0 || left > 800) break;
        __builtin_amdgcn_s_sleep(1);
      }
    }
  };
  auto poll = [&](auto& v, auto& off, unsigned tA, unsigned tB, auto nap, auto&& shadow) {
    constexpr int N = sizeof(v) / sizeof(v[0]);
    spin([&] {
      v4u t[N];
#pragma unroll
      for (int j = 0; j < N; ++j) t[j] = ld(off[j]);
      SCHED();
      shadow();
      SCHED();
      bool pend = false;
#pragma unroll
      for (int j = 0; j < N; ++j) {
        const bool hit = (t[j][1] == tA) & (t[j][3] == tB) & (off[j] != OOB);
        v[j][0] = hit ? t[j][0] : v[j][0];
        v[j][1] = hit ? t[j][2] : v[j][1];
        off[j] = hit ? OOB : off[j];
        pend = pend | (off[j] != OOB);
      }
      return __any(pend) == 0;
    }, nap);
  };
  auto gather = [&](auto& v, int base, unsigned tA, unsigned tB, auto nap, auto&& shadow) {
    constexpr int N = sizeof(v) / sizeof(v[0]);
    int off[N];
#pragma unroll
    for (int j = 0; j < N; ++j) { off[j] = base + lane * 16 + 1024 * j; v[j] = v2u{0u, 0u}; }
    poll(v, off, tA, tB, nap, shadow);
  };
  const int tq = (lane >> 2) & 3, tp = lane & 3;
  const v4u zero4 = {0u, 0u, 0u, 0u};
  const f32x4 zf = {0.f, 0.f, 0.f, 0.f};
  auto pick = [&](f32x4 a0, f32x4 a1, f32x4 a2, f32x4 a3) -> f32x4 {
    const f32x4 lo = (c & 2) ? a1 : a0, hi = (c & 2) ? a3 : a2;
    return (c & 4) ? hi : lo;
  };
  LAS float* smax = (LAS float*)(L + L_STAT);
  LAS float* ssum = smax + 32;

  if (wave == NCONS + 1) {
    const u64 ep = __hip_atomic_load((GAS u64*)(p.ws + WS_EPOCH), __ATOMIC_RELAXED, __HIP_MEMORY_SCOPE_AGENT);
    __builtin_amdgcn_s_barrier();
    const unsigned tA = rfl((unsigned)ep) + 1u, tB = rfl((unsigned)(ep >> 32)) + 1u;
    ctl[W_TAGA] = tA; ctl[W_TAGB] = tB;
    LDS_WAIT();
    ctl[W_TAGOK] = 1u;
    auto reduce = [&](auto nap) {
      v2u r[4];
      gather(r, WS_H2 + ((bid & 1) * 128 + (bid >> 1)) * 4096, tA, tB, nap, [] {});
      float s0 = (f(r[0][0]) + f(r[1][0])) + (f(r[2][0]) + f(r[3][0])), s1 = (f(r[0][1]) + f(r[1][1])) + (f(r[2][1]) + f(r[3][1]));
      s0 += dpp<ROR8>(s0); s1 += dpp<ROR8>(s1);
      s0 = xrow_sum(s0); s1 = xrow_sum(s1);
      if (lane < 8) {
        const int col = 32 * (bid >> 3) + 16 * ((lane >> 1) & 1) + 4 * ((bid >> 1) & 3) + 2 * (lane >> 2);
        *(GAS unsigned*)(p.out + (lane & 1) * 2048 + (bid & 1) * 1024 + col) = cvtpk(s0, s1);
      }
    };
    wait_ge(W_ORDY + W_PER, (unsigned)NCONS, IC<2>{});
    stamp(17);
    if (bid & 1) delay(D2);
    reduce(IC<0>{});
    if (bid == 1 && lane == 0)
      __hip_atomic_store((GAS u64*)(p.ws + WS_EPOCH), ((u64)tB << 32) | tA, __ATOMIC_RELAXED, __HIP_MEMORY_SCOPE_AGENT);
    if constexpr (STAMPS) {
      stamp(18);
      delay(300);
      if (lane < 32) stamps[lane] = lane == 0 ? t_start : *(volatile LAS u64*)(L + L_BYTES + lane * 8);
    }
    return;
  }

  if (wave > NCONS + 1) {
    const int tw = wave - NCONS - 2;
    __builtin_amdgcn_s_barrier();
    wait_ge(W_QPUB, 1u, IC<2>{});
    const unsigned tA = rfl(ctl[W_TAGA]), tB = rfl(ctl[W_TAGB]);
#pragma unroll 1
    for (int br = 0; br < 2; ++br) {
      delay(br ? DQB : QDELAY);
      v2u q[4];
      int off[4];
      const int qb = WS_Q + ((br * 4 + kvh) * 2 + b) * 16384 + tw * 2048 + lane * 16;
#pragma unroll
      for (int j = 0; j < 4; ++j) { off[j] = qb + (j >> 1) * 8192 + (j & 1) * 1024; q[j] = v2u{0u, 0u}; }
      poll(q, off, tA, tB, IC<4>{}, [] {});
      LAS unsigned* qi = (LAS unsigned*)(L + L_QIMG + br * 2048 + tw * 512 + lane * 4);
      qi[0] = cvtpk(f(q[0][0]) + f(q[2][0]), f(q[0][1]) + f(q[2][1]));
      qi[64] = cvtpk(f(q[1][0]) + f(q[3][0]), f(q[1][1]) + f(q[3][1]));
      LDS_WAIT();
      lds_add(W_QRDY + W_PER * br);
      if (tw == 0) stamp(15 + br);
    }
    v4u held = zero4;
#pragma unroll 1
    for (int br = 0; br < 2; ++br) {
      const int wb = W_PER * br, f0 = br ? F_WOB : F_WOA;
      const int o2 = c < 8 ? WS_H2 + (br * 128 + ((bid >> 5) * 4 + tw) * 4 + g) * 4096 + (bid & 31) * 128 + c * 16 : OOB;
      v4u wf[8];
      bool have_wo = false;
      auto try_wo = [&]() -> bool {
        if (rfl(ctl[W_FULL + ((f0 + 3) & (NSLOT - 1))]) < (unsigned)(f0 + 4)) return false;
        LDS_ACQ();
        const LAS unsigned char* s = L + L_RING + (f0 & (NSLOT - 1)) * SLOT + (8 * g + tq) * 256 + tw * 64 + tp * 8;
#pragma unroll
        for (int j = 0; j < 8; ++j) wf[j] = trfrag(s + (j >> 1) * 8192 + (j & 1) * 32, s + (j >> 1) * 8192 + (j & 1) * 32 + 1024);
        LDS_WAIT();
        PIN8(wf);
        unsigned old = 0u;
        if (lane == 0) old = __hip_atomic_fetch_add((LAS unsigned*)&ctl[W_WCNT + wb], 1u, __ATOMIC_RELAXED, __HIP_MEMORY_SCOPE_WORKGROUP);
        if (rfl(old) == 3u && lane < 4) ctl[W_FREE + ((f0 + lane) & (NSLOT - 1))] = (unsigned)(f0 + lane + 1);
        have_wo = true;
        return true;
      };
      v2u d[9];
      int off[9];
#pragma unroll
      for (int j = 0; j < 8; ++j) { off[j] = WS_H1 + ((br * 32 + (bid & 31)) * 4 + tw) * 8192 + lane * 16 + 1024 * j; d[j] = v2u{0u, 0u}; }
      off[8] = WS_ML + (br * 8 + (tw >> 1) * 4 + ((bid >> 3) & 3)) * 4096 + (4 * (c & 7) + g) * 128 + (bid & 7) * 16;
      d[8] = v2u{0u, 0u};
      if (br == 0) {
        spin(try_wo, IC<16>{});
        wait_ge(W_MCNT + (ALATE ? W_PER : 0), (unsigned)(ALATE ? 1 : NCONS), IC<8>{});
        if (!ALATE) delay(DH1);
        poll(d, off, tA, tB, IC<(ALATE ? 0 : 16)>{}, [] {});
      } else {
        wait_ge(W_MCNT + wb, (unsigned)NCONS, IC<2>{});
        if (HELD == 0) { hold(0); st(held, o2 - 128 * 4096); }
        hold(D1);
        bool sent = HELD != 1;
        poll(d, off, tA, tB, IC<0>{}, [&] { if (!sent) { st(held, o2 - 128 * 4096); sent = true; } if (!have_wo) try_wo(); });
        if (HELD == 2) st(held, o2 - 128 * 4096);
      }
      if (br == 0 && tw == 0) {
        const unsigned mine = rfl(ctl[W_STAMP]);
        float lag = (float)((int)((mine & ~0xfffu) - (d[8][1] & ~0xfffu)) >> 12), dur = (float)(d[8][1] & 0xfffu);
        lag += dpp<ROR1>(lag); lag += dpp<ROR2>(lag); lag += dpp<ROR4>(lag);
        dur += dpp<ROR1>(dur); dur += dpp<ROR2>(dur); dur += dpp<ROR4>(dur);
        const int ticks = (int)f(rfl(u(xrow_sum(lag)))) / 32;
        const float per = f(rfl(u(xrow_sum(dur)))) * (1.0f / 1024.0f);
        const unsigned now = (unsigned)rt();
        const unsigned mean = now - (unsigned)((int)((now << 12) - (mine & ~0xfffu)) >> 12) - (unsigned)ticks;
        const unsigned tau = (unsigned)(per * (0.256f * TPM));
        if (SOFF > -1000) *(volatile LAS v2u*)&ctl[W_BASE] = v2u{mean + (unsigned)SOFF - ((unsigned)F_WOA * tau >> 8), tau};
        ctl[W_HOLD] = ((mean + (unsigned)((int)(per * 68.0f) + HOFF)) << 12) | 1u;
        if constexpr (STAMPS) { if (lane == 0) {
          *(volatile LAS u64*)(L + L_BYTES + 14 * 8) = rt() + (u64)(long long)((int)(ctl[W_HOLD] - ((unsigned)rt() << 12)) >> 12); } }
      }
      float M = f(d[8][0]);
      M = fmaxf(M, dpp<ROR1>(M)); M = fmaxf(M, dpp<ROR2>(M)); M = fmaxf(M, dpp<ROR4>(M));
      M = xrow_max(M);
      const float w = __builtin_amdgcn_exp2f(f(d[8][0]) - M);
      float S = w;
      S += dpp<ROR1>(S); S += dpp<ROR2>(S); S += dpp<ROR4>(S);
      const float inv = 1.0f / xrow_sum(S);
      f32x4 o = zf;
#define ACC(J) o += f32x4{f(d[J][0] << 16), f(d[J][0] & 0xffff0000u), f(d[J][1] << 16), f(d[J][1] & 0xffff0000u)} * dpp<BCAST + J>(w);
      ACC(0) ACC(1) ACC(2) ACC(3) ACC(4) ACC(5) ACC(6) ACC(7)
#undef ACC
      const v2u pk = {cvtpk(xrow_sum(o[0]) * inv, xrow_sum(o[1]) * inv), cvtpk(xrow_sum(o[2]) * inv, xrow_sum(o[3]) * inv)};
      if (lane < 16) *(LAS v2u*)(L + L_OIMG + br * 512 + tw * 128 + lane * 8) = pk;
      LDS_WAIT();
      lds_add(W_ORDY + wb);
      if (tw == 0 && br == 1) stamp(1);
      if (!have_wo) spin(try_wo, IC<0>{});
      wait_ge(W_ORDY + wb, (unsigned)NCONS, IC<0>{});
      f32x4 a0 = zf, a1 = zf;
#pragma unroll
      for (int ks = 0; ks < 4; ++ks) {
        const v4u of = *(const LAS v4u*)(L + L_OIMG + br * 512 + (c & 1) * 256 + ks * 64 + g * 16);
        a0 = mfma(wf[2 * ks], of, a0);
        a1 = mfma(wf[2 * ks + 1], of, a1);
      }
      const f32x4 r = (c & 2) ? a1 : a0;
      held = v4u{u((c & 4) ? r[2] : r[0]), tA, u((c & 4) ? r[3] : r[1]), tB};
      if (br == 1 || ALATE) st(held, o2);
      if (tw == 0) stamp(br ? 7 : 22);
    }
    return;
  }

  const int ci = wave - 1;
  v4u xf[4];
#pragma unroll
  for (int ks = 0; ks < 4; ++ks) xf[ks] = *(const GAS v4u*)(p.x[bid >> 7] + (c & 1) * 1024 + ((bid >> 6) & 1) * 512 + ci * 128 + ks * 32 + 8 * g);
  __builtin_amdgcn_s_barrier();
  vmwait<0>();
  {
    const LAS unsigned char* s = L + L_RING + ci * 2 * SLOT + (8 * g + tq) * 128 + tp * 8;
    v4u w0[8], w1[8];
#pragma unroll
    for (int j = 0; j < 8; ++j) {
      const int o = (j >> 2) * 4096 + (j & 3) * 32;
      w0[j] = trfrag(s + o, s + o + 512); w1[j] = trfrag(s + 8192 + o, s + 8192 + o + 512);
    }
    LDS_WAIT();
    if (lane < 2) ctl[W_FREE + 2 * ci + lane] = (unsigned)(2 * ci + lane + 1);
    PIN8(w0); PIN8(w1);
    f32x4 a[4];
#pragma unroll
    for (int e = 0; e < 4; ++e) a[e] = mfma(w1[4 + e], xf[3], mfma(w1[e], xf[2], mfma(w0[4 + e], xf[1], mfma(w0[e], xf[0], zf))));
    const f32x4 r = pick(a[0], a[1], a[2], a[3]);
    if (c < 8) *(LAS f32x4*)(L + L_QRED + ci * 512 + (c & 1) * 256 + (c >> 1) * 64 + g * 16) = r;
    LDS_WAIT();
    lds_add(W_QCNT);
  }
  wait_ge(W_TAGOK, 1u, IC<0>{});
  const unsigned tA = rfl(ctl[W_TAGA]), tB = rfl(ctl[W_TAGB]);
  if (ci == 0) {
    wait_ge(W_QCNT, (unsigned)NCONS, IC<0>{});
    f32x2 q = {0.f, 0.f};
#pragma unroll
    for (int w = 0; w < NCONS; ++w) q += *(const LAS f32x2*)(L + L_QRED + w * 512 + lane * 8);
    st(v4u{u(q[0]), tA, u(q[1]), tB},
       WS_Q + ((((bid >> 7) * 4 + ((bid >> 4) & 3)) * 2 + (lane >> 5)) * 2 + ((bid >> 6) & 1)) * 8192 + (bid & 15) * 512 + (lane & 31) * 16);
    ctl[W_QPUB] = 1u;
  }
  if (ci == 0) stamp(3);

  v4u keep = zero4, keepl = zero4;
#pragma unroll 1
  for (int br = 0; br < 2; ++br) {
    const int wb = W_PER * br, fk = (br ? F_KB : F_KA) + ci, fv = (br ? F_VB : F_VA) + ci;
    f32x4 sc[TPC][2];
    v4u qf[4];
    unsigned kr = (unsigned)c * 256u + (unsigned)((g ^ c) * 16);
    asm volatile("" : "+v"(kr));
    auto ktile = [&](auto tc) {
      constexpr int t = decltype(tc)::value;
      const int fl = fk + NCONS * t, slot = fl & (NSLOT - 1);
      wait_ge(W_FULL + slot, (unsigned)(fl + 1), IC<1>{});
      v4u K[8];
#pragma unroll
      for (int j = 0; j < 8; ++j) K[j] = *(const LAS v4u*)(L + L_RING + ((kr + slot * SLOT) ^ (64u * (j & 3))) + (j >> 2) * 4096);
      LDS_WAIT();
      ctl[W_FREE + slot] = (unsigned)(fl + 1);
      PIN8(K);
      if constexpr (t == 0) {
        wait_ge(W_QRDY + wb, (unsigned)NCONS, IC<1>{});
#pragma unroll
        for (int j = 0; j < 4; ++j) {
          const v4u q = *(const LAS v4u*)(L + L_QIMG + br * 2048 + (c & 7) * 256 + j * 64 + g * 16);
          qf[j] = c < 8 ? q : zero4;
        }
        if (ci == 0 && br == 0) stamp(4);
      }
      f32x4 s0 = zf, s1 = zf;
#pragma unroll
      for (int j = 0; j < 4; ++j) { s0 = mfma(K[j], qf[j], s0); s1 = mfma(K[4 + j], qf[j], s1); }
      sc[t][0] = s0; sc[t][1] = s1;
    };
    each<0, TPC>(ktile);
    {
      float m = -INFINITY;
#pragma unroll
      for (int t = 0; t < TPC; ++t)
#pragma unroll
        for (int j = 0; j < 2; ++j) m = fmaxf(m, fmaxf(fmaxf(sc[t][j][0], sc[t][j][1]), fmaxf(sc[t][j][2], sc[t][j][3])));
      m = xrow_max(m) * C2;
      if (lane < 8) smax[ci * 8 + lane] = m;
      LDS_WAIT();
      lds_add(W_XCNT + wb);
    }
    wait_ge(W_XCNT + wb, (unsigned)NCONS, IC<0>{});
    v4u pf[TPC];
    {
      const int h = c & 7;
      const float m = fmaxf(fmaxf(smax[h], smax[8 + h]), fmaxf(smax[16 + h], smax[24 + h]));
      float l = 0.f;
#pragma unroll
      for (int t = 0; t < TPC; ++t) {
        float pr[8];
#pragma unroll
        for (int i = 0; i < 8; ++i) pr[i] = __builtin_amdgcn_exp2f(fmaf(sc[t][i >> 2][i & 3], C2, -m));
        pf[t] = v4u{cvtpk(pr[0], pr[1]), cvtpk(pr[2], pr[3]), cvtpk(pr[4], pr[5]), cvtpk(pr[6], pr[7])};
        l += ((pr[0] + pr[1]) + (pr[2] + pr[3])) + ((pr[4] + pr[5]) + (pr[6] + pr[7]));
      }
      l = xrow_sum(l);
      if (lane < 8) ssum[ci * 8 + lane] = l;
      LDS_WAIT();
      lds_add(W_LCNT + wb);
    }
    const int h = 2 * ci + (lane >> 5), l5 = lane & 31;
    wait_ge(W_LCNT + wb, (unsigned)NCONS, IC<0>{});
    const float inv = 1.0f / ((ssum[h] + ssum[8 + h]) + (ssum[16 + h] + ssum[24 + h]));
    const int h8 = lane & 7;
    const float lse = fmaxf(fmaxf(smax[h8], smax[8 + h8]), fmaxf(smax[16 + h8], smax[24 + h8]))
                    + __builtin_amdgcn_logf((ssum[h8] + ssum[8 + h8]) + (ssum[16 + h8] + ssum[24 + h8]));
    f32x4 acc[8];
#pragma unroll
    for (int e = 0; e < 8; ++e) acc[e] = zf;
    unsigned vr = (unsigned)(4 * g + tq) * 256u + (unsigned)((4 * (g & 1) + tq) * 32 + tp * 8);
    asm volatile("" : "+v"(vr));
    auto vtile = [&](auto tc) {
      constexpr int t = decltype(tc)::value;
      const int fl = fv + NCONS * t, slot = fl & (NSLOT - 1);
      wait_ge(W_FULL + slot, (unsigned)(fl + 1), IC<1>{});
      if (t == TPC - 1 && ci == 3 && br == 1) stamp(19);
      v4u V[8];
#pragma unroll
      for (int e = 0; e < 8; ++e) { const LAS unsigned char* s = L + L_RING + ((vr + slot * SLOT) ^ (32u * e)); V[e] = trfrag(s, s + 4096); }
      LDS_WAIT();
      ctl[W_FREE + slot] = (unsigned)(fl + 1);
      PIN8(V);
#pragma unroll
      for (int e = 0; e < 8; ++e) acc[e] = mfma(V[e], pf[t], acc[e]);
    };
    each<0, TPC>(vtile);
    if (c < 8) {
      LAS unsigned char* row = L + L_MRG + (ci * 8 + c) * MROW + g * 16;
#pragma unroll
      for (int e = 0; e < 8; ++e) *(LAS f32x4*)(row + e * 64) = acc[e];
    }
    LDS_WAIT();
    lds_add(W_MCNT + wb);
    const int rec = WS_H1 + ((br * 32 + kvh * 8 + h) * 4 + b * 2 + (l5 >> 4)) * 8192 + part * 256 + (l5 & 15) * 16;
    const int mlo = lane < 8 && ci == 0 ? WS_ML + (br * 8 + sidx) * 4096 + part * 128 + lane * 16 : OOB;
    if (ALATE && br == 1) { st(keep, rec - 32 * 4 * 8192); st(keepl, mlo == OOB ? OOB : mlo - 8 * 4096); }
    wait_ge(W_MCNT + wb, (unsigned)NCONS, IC<0>{});
    const LAS unsigned char* mr = L + L_MRG + h * MROW + l5 * 16;
    const f32x4 o = ((*(const LAS f32x4*)mr + *(const LAS f32x4*)(mr + 8 * MROW)) + (*(const LAS f32x4*)(mr + 16 * MROW) + *(const LAS f32x4*)(mr + 24 * MROW))) * inv;
    keep = v4u{cvtpk(o[0], o[1]), tA, cvtpk(o[2], o[3]), tB};
    keepl = v4u{u(lse), tA, ctl[W_STAMP], tB};
    if (br == 1) hold(0);
    if (!ALATE || br == 1) { st(keep, rec); st(keepl, mlo); }
    if (ci == 3) stamp(br ? 6 : 5);
  }
}

template <int SOFF, int HOFF, int BEH, int LEAD, int WX, int STAMPS>
static void launch(void* const* d_in, void* d_out, void* d_ws, hipStream_t stream) {
  Params p;
  for (int br = 0; br < 2; ++br) {
    p.x[br] = (const u16*)d_in[5 * br + 0]; p.k[br] = (const u16*)d_in[5 * br + 1]; p.v[br] = (const u16*)d_in[5 * br + 2];
    p.wq[br] = (const u16*)d_in[5 * br + 3]; p.wo[br] = (const u16*)d_in[5 * br + 4];
  }
  p.out = (u16*)d_out; p.ws = (unsigned char*)d_ws;
  constexpr int bytes = L_BYTES + (STAMPS ? 256 : 0);
  (void)hipFuncSetAttribute((const void*)k_fused<SOFF, HOFF, BEH, LEAD, WX, STAMPS>, hipFuncAttributeMaxDynamicSharedMemorySize, bytes);
  hipLaunchKernelGGL((k_fused<SOFF, HOFF, BEH, LEAD, WX, STAMPS>), dim3(NWG), dim3(64 * NWAVE), bytes, stream, p);
}

}

extern "C" void kernel_launch(void* const* d_in, const int* in_sizes, int n_in, void* d_out, int out_size, void* d_ws, size_t ws_size,
                              hipStream_t stream) {
  fused::launch<CFG>(d_in, d_out, d_ws, stream);
}
```

```cpp
#include <hip/hip_runtime.h>
#include <hip/hip_bf16.h>
#include <cstdint>

#define GAS __attribute__((address_space(1)))
#define LAS __attribute__((address_space(3)))
typedef unsigned v4u __attribute__((ext_vector_type(4)));
typedef unsigned v2u __attribute__((ext_vector_type(2)));
typedef float f32x4 __attribute__((ext_vector_type(4)));
typedef float f32x2 __attribute__((ext_vector_type(2)));
typedef short bf16x8 __attribute__((ext_vector_type(8)));
typedef short s16x4 __attribute__((ext_vector_type(4)));
typedef unsigned short u16;
typedef unsigned long long u64;

#define CFG 80, 250, 1, 70, 60, 0

namespace fused {

constexpr int SK = 32768, DH = 128;
constexpr int NWG = 256, NCONS = 4, NWAVE = 10;
constexpr int NSLOT = 16, SLOT = 8192;
constexpr int NT = 32, TPC = NT / NCONS;
constexpr int F_KA = 8, F_VA = F_KA + NT;
constexpr float C2 = 0.088388347648318440f * 1.4426950408889634f;
constexpr u64 TCAP = 2000000;
constexpr int OOB = 0x7ffffff0;

constexpr int WS_EPOCH = 0;
constexpr int WS_ML = 4096;
constexpr int WS_Q = WS_ML + 2 * 8 * 4096;
constexpr int WS_H2 = WS_Q + 2 * 8 * 16384;
constexpr int WS_H1 = WS_H2 + 256 * 4096;
constexpr int WS_END = WS_H1 + 2 * 128 * 8192;
constexpr int WS_STAMP = WS_END;

constexpr int L_RING = 0;
constexpr int L_CTL = NSLOT * SLOT;
constexpr int L_QIMG = L_CTL + 512;
constexpr int L_QRED = L_QIMG + 4096;
constexpr int L_OIMG = L_QRED + 2048;
constexpr int L_STAT = L_OIMG + 1024;
constexpr int MROW = 528;
constexpr int L_MRG = L_STAT + 512;
constexpr int L_BYTES = L_MRG + 4 * 8 * MROW;
enum { W_FULL = 0, W_FREE = 16, W_ABORT = 32, W_TAGA, W_TAGB, W_TAGOK, W_STAMP, W_HOLD, W_BASE, W_TAU,
       W_QCNT = 40, W_QPUB, W_QRDY, W_XCNT, W_LCNT, W_SRDY, W_MCNT, W_ORDY, W_WCNT, W_PER = 16 };
static_assert(L_BYTES <= 160 * 1024 && L_BYTES > 80 * 1024, "one workgroup per CU");

struct Params { const u16* x[2]; const u16* k[2]; const u16* v[2]; const u16* wq[2]; const u16* wo[2]; u16* out; unsigned char* ws; };

__device__ __forceinline__ u64 rt() { return __builtin_amdgcn_s_memrealtime(); }
__device__ __forceinline__ unsigned rfl(unsigned v) { return (unsigned)__builtin_amdgcn_readfirstlane((int)v); }
__device__ __forceinline__ unsigned cvtpk(float lo, float hi) {
  unsigned r; asm("v_cvt_pk_bf16_f32 %0, %1, %2" : "=v"(r) : "v"(lo), "v"(hi)); return r;
}
__device__ __forceinline__ f32x4 mfma(v4u a, v4u b, f32x4 c) {
  return __builtin_amdgcn_mfma_f32_16x16x32_bf16(__builtin_bit_cast(bf16x8, a), __builtin_bit_cast(bf16x8, b), c, 0, 0, 0);
}
__device__ __forceinline__ float f(unsigned u) { return __uint_as_float(u); }
__device__ __forceinline__ unsigned u(float x) { return __float_as_uint(x); }
__device__ __forceinline__ float xrow_max(float x) {
  auto s = __builtin_amdgcn_permlane16_swap(u(x), u(x), false, false);
  x = fmaxf(f(s[0]), f(s[1]));
  auto t = __builtin_amdgcn_permlane32_swap(u(x), u(x), false, false);
  return fmaxf(f(t[0]), f(t[1]));
}
__device__ __forceinline__ float xrow_sum(float x) {
  auto s = __builtin_amdgcn_permlane16_swap(u(x), u(x), false, false);
  x = f(s[0]) + f(s[1]);
  auto t = __builtin_amdgcn_permlane32_swap(u(x), u(x), false, false);
  return f(t[0]) + f(t[1]);
}
template <int CTRL> __device__ __forceinline__ float dpp(float x) {
  return __builtin_bit_cast(float, __builtin_amdgcn_mov_dpp(__builtin_bit_cast(int, x), CTRL, 0xf, 0xf, true));
}
constexpr int ROR1 = 0x121, ROR2 = 0x122, ROR4 = 0x124, ROR8 = 0x128, BCAST = 0x150;
template <int N> struct IC { static constexpr int value = N; };
template <int I, int E, class F> __device__ __forceinline__ void each(F& fn) {
  if constexpr (I < E) { fn(IC<I>{}); each<I + 1, E>(fn); }
}
template <int N> __device__ __forceinline__ void vmwait() { asm volatile("s_waitcnt vmcnt(%0)" ::"i"(N) : "memory"); }
#define LDS_WAIT() asm volatile("s_waitcnt lgkmcnt(0)" ::: "memory")
#define LDS_ACQ() __builtin_amdgcn_fence(__ATOMIC_ACQUIRE, "workgroup")
#define SCHED() __builtin_amdgcn_sched_barrier(0)
#define PIN8(w) asm volatile("" : "+v"(w[0]), "+v"(w[1]), "+v"(w[2]), "+v"(w[3]), "+v"(w[4]), "+v"(w[5]), "+v"(w[6]), "+v"(w[7]) : : "memory")
#define GLDS(src, dst, imm) __builtin_amdgcn_global_load_lds((const GAS unsigned*)(src), (LAS unsigned*)(dst), 16, (imm), 2)

__device__ __forceinline__ v4u trfrag(const LAS unsigned char* lo, const LAS unsigned char* hi) {
  const v2u a = __builtin_bit_cast(v2u, __builtin_amdgcn_ds_read_tr16_b64_v4i16((LAS s16x4*)lo));
  const v2u b = __builtin_bit_cast(v2u, __builtin_amdgcn_ds_read_tr16_b64_v4i16((LAS s16x4*)hi));
  return v4u{a[0], a[1], b[0], b[1]};
}

template <int SOFF, int HOFF, int WOL, int D1, int D2, int STAMPS>
__global__ void __launch_bounds__(64 * NWAVE) k_fused(Params p) {
  constexpr int DQB = 600, QDELAY = 100, RT = 5, STAG = 0, WN = 16, HALF = 0, ALATE = 0, TPM = 1000, DH1 = 250, NARROW = 0, HELD = 0, BEH = 20, LEAD = 30, WX = 16;
  static_assert(WOL == 1, "");
  constexpr int F_KB = F_VA + NT + (WOL ? 0 : 4), F_VB = F_KB + NT, F_KVEND = F_VB + NT, F_WOA = WOL ? F_KVEND : F_VA + NT, F_WOB = F_KVEND + (WOL ? 4 : 0);
  static_assert(WN % 4 == 0 && (HALF || WN % 8 == 0) && WN >= 8 && WN <= 48 && RT >= 0 && RT <= 5, "pieces left in flight; run of 2^RT tiles");
  extern __shared__ __attribute__((aligned(16))) unsigned char lds_raw[];
  LAS unsigned char* L = (LAS unsigned char*)lds_raw;
  volatile LAS unsigned* ctl = (volatile LAS unsigned*)(L + L_CTL);
  const u64 t_start = rt();
  const int bid = blockIdx.x, sidx = bid >> 5, part = bid & 31, b = sidx >> 2, kvh = sidx & 3;
  const int tid = threadIdx.x, lane = tid & 63, wave = (int)rfl(tid >> 6);
  const int c = lane & 15, g = lane >> 4;
  if (__builtin_expect(wave >= 1 && wave <= NCONS, 1)) {
    const unsigned char* wq = (const unsigned char*)p.wq[bid >> 7] + (size_t)(((bid >> 6) & 1) * 512 + (wave - 1) * 128) * 8192 + (bid & 63) * 128;
    const unsigned wq_off = (unsigned)(lane >> 3) * 8192u + (unsigned)(lane & 7) * 16u;
#pragma unroll
    for (int j = 0; j < 16; ++j) GLDS(wq + (size_t)j * 8 * 8192 + wq_off, L + L_RING + (wave - 1) * 2 * SLOT + j * 1024, 0);
  }
  u64* stamps = (u64*)(p.ws + WS_STAMP) + bid * 32;
  auto stamp = [&](int i) { if constexpr (STAMPS) { const u64 t = rt(); if (lane == 0) *(volatile LAS u64*)(L + L_BYTES + i * 8) = t; } };

  auto spin = [&](auto&& cond, auto nap) {
    for (unsigned it = 1; !cond(); ++it) {
      if constexpr (decltype(nap)::value > 0) __builtin_amdgcn_s_sleep(decltype(nap)::value);
      if ((it & 63u) == 0u) {
        if (rt() - t_start > TCAP) ctl[W_ABORT] = 1u;
        if (rfl(ctl[W_ABORT])) break;
      }
    }
  };
  auto wait_ge = [&](int w, unsigned n, auto nap) {
    spin([&] { return rfl(ctl[w]) >= n; }, nap);
    LDS_ACQ();
  };

  if (__builtin_expect(wave == 0, 1)) {
    __builtin_amdgcn_s_setprio(3);
    ctl[lane] = 0u; ctl[64 + lane] = 0u;
    unsigned koff[4], voff[2];
#pragma unroll
    for (int j = 0; j < 4; ++j) koff[j] = (unsigned)g * 256u + (unsigned)((c ^ (4 * j + g)) * 16);
#pragma unroll
    for (int j = 0; j < 2; ++j) voff[j] = (unsigned)g * 256u + (unsigned)((c ^ (2 * (4 * j + g))) * 16);
    constexpr int LAG = WN / 8 + 1;
    int fill = F_KA, lastpark = 0;
    u64 parked = 0;
    unsigned fr = 0u;
    auto full = [&](int fl) { ctl[W_FULL + (fl & (NSLOT - 1))] = (unsigned)(fl + 1); };
    auto drain = [&]() {
      static_assert(LAG + WX / 8 <= 6, "");
      vmwait<40>(); full(fill - 6); vmwait<32>(); full(fill - 5); vmwait<24>(); full(fill - 4);
      vmwait<16>(); full(fill - 3); vmwait<8>(); full(fill - 2); vmwait<0>(); full(fill - 1);
    };
    auto room = [&]() {
      if (rfl(fr) < (unsigned)(fill - NSLOT + 1) && fill >= NSLOT) {
        const u64 tp = rt();
        drain();
        spin([&] { return rfl(ctl[W_FREE + (fill & (NSLOT - 1))]) >= (unsigned)(fill - NSLOT + 1); }, IC<1>{});
        parked += rt() - tp; lastpark = fill;
      }
    };
    v2u sched = {0u, 0u};
    unsigned now = 0u, nwide = 0u;
    auto issue = [&](const unsigned char* s0, const unsigned char* s1, unsigned o0, unsigned o1, unsigned o2, unsigned o3) {
      const unsigned dst = (unsigned)(size_t)(L + L_RING) + (unsigned)((fill & (NSLOT - 1)) * SLOT);
      room();
      bool wide = false;
      const unsigned tau = rfl(sched[1]);
      if (tau != 0u) {
        const unsigned due = rfl(sched[0]) + ((unsigned)fill * tau >> 8);
        int late = (int)(now - due);
        wide = late > BEH && fill < F_KVEND - NARROW;
        while (late < -LEAD && late > -1000) { __builtin_amdgcn_s_sleep(1); late = (int)((unsigned)rt() - due); }
      }
#define FILL(N) asm volatile("s_mov_b32 m0, %0\n\ts_waitcnt vmcnt(%7)\n\t"                                                     \
                   "global_load_lds_dwordx4 %3, %1 nt\n\tglobal_load_lds_dwordx4 %4, %1 offset:1024 nt\n\t"                    \
                   "global_load_lds_dwordx4 %5, %1 offset:2048 nt\n\tglobal_load_lds_dwordx4 %6, %1 offset:3072 nt\n\t"        \
                   "s_add_u32 m0, %0, 0x1000\n\ts_nop 0\n\t"                                                                  \
                   "global_load_lds_dwordx4 %3, %2 nt\n\tglobal_load_lds_dwordx4 %4, %2 offset:1024 nt\n\t"                    \
                   "global_load_lds_dwordx4 %5, %2 offset:2048 nt\n\tglobal_load_lds_dwordx4 %6, %2 offset:3072 nt"            \
                   :: "s"(dst), "s"(s0), "s"(s1), "v"(o0), "v"(o1), "v"(o2), "v"(o3), "i"(N) : "memory", "scc")
      if (wide) {
        ++nwide;
        FILL(WN + WX);
        full(fill - LAG - WX / 8);
      } else {
        FILL(WN);
#pragma unroll
        for (int j = WX / 8; j >= 0; --j) full(fill - LAG - j);
      }
#undef FILL
      ++fill;
      fr = ctl[W_FREE + (fill & (NSLOT - 1))];
      sched = *(volatile LAS v2u*)&ctl[W_BASE];
      now = (unsigned)rt();
    };
    const size_t sbase = (size_t)sidx * SK * DH * 2;
    auto tile_off = [&](int i) -> size_t {
      constexpr int NR = NT >> RT;
      return (size_t)((((((i >> RT) + STAG * sidx) & (NR - 1)) * 32 + part) << RT) | (i & ((1 << RT) - 1))) * 8192;
    };
    const size_t wo_base = (size_t)((bid & 31) * 128) * 2048 + (bid >> 5) * 256;
    const unsigned woff = (unsigned)g * 2048u + (unsigned)c * 16u;
    LDS_WAIT();
    __builtin_amdgcn_s_barrier();
    stamp(11);
#pragma unroll 1
    for (int br = 0; br < 2; ++br) {
      const unsigned char* kb = (const unsigned char*)p.k[br] + sbase;
      const unsigned char* vb = (const unsigned char*)p.v[br] + sbase;
#pragma unroll 1
      for (int i = 0; i < NT; ++i) { const unsigned char* s = kb + tile_off(i); issue(s, s + 4096, koff[0], koff[1], koff[2], koff[3]); }
      stamp(24 + 2 * br);
      const unsigned tk = (unsigned)rt();
#pragma unroll 1
      for (int i = 0; i < NT; ++i) { const unsigned char* s = vb + tile_off(i); issue(s, s + 4096, voff[0], voff[1], voff[0], voff[1]); }
      stamp(25 + 2 * br);
      if (br == 0) { const unsigned tv = (unsigned)rt(); ctl[W_STAMP] = (tv << 12) | ((tv - tk) & 0xfffu); }
#pragma unroll 1
      for (int i = 0; i < (WOL ? 8 * br : 4); ++i) {
        const unsigned char* s = (const unsigned char*)p.wo[WOL ? i >> 2 : br] + wo_base + (size_t)((i & 3) * 32) * 2048;
        issue(s, s + 4 * 8192, woff, woff + 7168u, woff + 14336u, woff + 21504u);
      }
    }
    drain();
    stamp(9);
    if constexpr (STAMPS) { if (lane == 0) *(volatile LAS u64*)(L + L_BYTES + 23 * 8) = (u64)nwide * 100; }
    if constexpr (STAMPS) { if (lane == 0) { *(volatile LAS u64*)(L + L_BYTES + 20 * 8) = parked; *(volatile LAS u64*)(L + L_BYTES + 21 * 8) = (u64)lastpark * 100; } }
    return;
  }

  const __amdgpu_buffer_rsrc_t rs = __builtin_amdgcn_make_buffer_rsrc(p.ws, 0, WS_END, 0x00020000);
  auto ld = [&](int off) -> v4u { return __builtin_amdgcn_raw_buffer_load_b128(rs, off, 0, 16); };
  auto st = [&](v4u v, int off) { __builtin_amdgcn_raw_buffer_store_b128(v, rs, off, 0, 16); };
  auto lds_add = [&](int w) { if (lane == 0) __hip_atomic_fetch_add((LAS unsigned*)&ctl[w], 1u, __ATOMIC_RELAXED, __HIP_MEMORY_SCOPE_WORKGROUP); };
  auto delay = [&](u64 ticks) { const u64 t0 = rt(); while (rt() - t0 < ticks) __builtin_amdgcn_s_sleep(2); };
  auto hold = [&](int extra) {
    const unsigned h = rfl(ctl[W_HOLD]);
    if (HOFF > -1000 && h != 0u) {
      for (;;) {
        const int left = ((int)(h - ((unsigned)rt() << 12)) >> 12) + extra;
        if (left <= 0 || left > 800) break;
        __builtin_amdgcn_s_sleep(1);
      }
    }
  };
  auto poll = [&](auto& v, auto& off, unsigned tA, unsigned tB, auto nap, auto&& shadow) {
    constexpr int N = sizeof(v) / sizeof(v[0]);
    spin([&] {
      v4u t[N];
#pragma unroll
      for (int j = 0; j < N; ++j) t[j] = ld(off[j]);
      SCHED();
      shadow();
      SCHED();
      bool pend = false;
#pragma unroll
      for (int j = 0; j < N; ++j) {
        const bool hit = (t[j][1] == tA) & (t[j][3] == tB) & (off[j] != OOB);
        v[j][0] = hit ? t[j][0] : v[j][0];
        v[j][1] = hit ? t[j][2] : v[j][1];
        off[j] = hit ? OOB : off[j];
        pend = pend | (off[j] != OOB);
      }
      return __any(pend) == 0;
    }, nap);
  };
  auto gather = [&](auto& v, int base, unsigned tA, unsigned tB, auto nap, auto&& shadow) {
    constexpr int N = sizeof(v) / sizeof(v[0]);
    int off[N];
#pragma unroll
    for (int j = 0; j < N; ++j) { off[j] = base + lane * 16 + 1024 * j; v[j] = v2u{0u, 0u}; }
    poll(v, off, tA, tB, nap, shadow);
  };
  const int tq = (lane >> 2) & 3, tp = lane & 3;
  const v4u zero4 = {0u, 0u, 0u, 0u};
  const f32x4 zf = {0.f, 0.f, 0.f, 0.f};
  auto pick = [&](f32x4 a0, f32x4 a1, f32x4 a2, f32x4 a3) -> f32x4 {
    const f32x4 lo = (c & 2) ? a1 : a0, hi = (c & 2) ? a3 : a2;
    return (c & 4) ? hi : lo;
  };
  LAS float* smax = (LAS float*)(L + L_STAT);
  LAS float* ssum = smax + 32;

  auto wo_ready = [&](int f0) { return rfl(ctl[W_FULL + ((f0 + 3) & (NSLOT - 1))]) >= (unsigned)(f0 + 4); };
  auto wo_frags = [&](v4u (&wf)[8], int f0, int w) {
    LDS_ACQ();
    const LAS unsigned char* s = L + L_RING + (f0 & (NSLOT - 1)) * SLOT + (8 * g + tq) * 256 + w * 64 + tp * 8;
#pragma unroll
    for (int j = 0; j < 8; ++j) wf[j] = trfrag(s + (j >> 1) * 8192 + (j & 1) * 32, s + (j >> 1) * 8192 + (j & 1) * 32 + 1024);
  };
  auto project = [&](const v4u (&wf)[8], int br, int w, unsigned tA, unsigned tB) {
    const int o2 = c < 8 ? WS_H2 + (br * 128 + ((bid >> 5) * 4 + w) * 4 + g) * 4096 + (bid & 31) * 128 + c * 16 : OOB;
    wait_ge(W_ORDY + W_PER * br, (unsigned)NCONS, IC<0>{});
    f32x4 a0 = zf, a1 = zf;
#pragma unroll
    for (int ks = 0; ks < 4; ++ks) {
      const v4u of = *(const LAS v4u*)(L + L_OIMG + br * 512 + (c & 1) * 256 + ks * 64 + g * 16);
      a0 = mfma(wf[2 * ks], of, a0);
      a1 = mfma(wf[2 * ks + 1], of, a1);
    }
    const f32x4 r = (c & 2) ? a1 : a0;
    st(v4u{u((c & 4) ? r[2] : r[0]), tA, u((c & 4) ? r[3] : r[1]), tB}, o2);
  };

  if (wave == NCONS + 1) {
    const u64 ep = __hip_atomic_load((GAS u64*)(p.ws + WS_EPOCH), __ATOMIC_RELAXED, __HIP_MEMORY_SCOPE_AGENT);
    __builtin_amdgcn_s_barrier();
    const unsigned tA = rfl((unsigned)ep) + 1u, tB = rfl((unsigned)(ep >> 32)) + 1u;
    ctl[W_TAGA] = tA; ctl[W_TAGB] = tB;
    LDS_WAIT();
    ctl[W_TAGOK] = 1u;
    auto reduce = [&](auto nap) {
      v2u r[4];
      gather(r, WS_H2 + ((bid & 1) * 128 + (bid >> 1)) * 4096, tA, tB, nap, [] {});
      float s0 = (f(r[0][0]) + f(r[1][0])) + (f(r[2][0]) + f(r[3][0])), s1 = (f(r[0][1]) + f(r[1][1])) + (f(r[2][1]) + f(r[3][1]));
      s0 += dpp<ROR8>(s0); s1 += dpp<ROR8>(s1);
      s0 = xrow_sum(s0); s1 = xrow_sum(s1);
      if (lane < 8) {
        const int col = 32 * (bid >> 3) + 16 * ((lane >> 1) & 1) + 4 * ((bid >> 1) & 3) + 2 * (lane >> 2);
        *(GAS unsigned*)(p.out + (lane & 1) * 2048 + (bid & 1) * 1024 + col) = cvtpk(s0, s1);
      }
    };
    wait_ge(W_ORDY + W_PER, (unsigned)NCONS, IC<2>{});
    stamp(17);
    if (bid & 1) delay(D2);
    reduce(IC<0>{});
    if (bid == 1 && lane == 0)
      __hip_atomic_store((GAS u64*)(p.ws + WS_EPOCH), ((u64)tB << 32) | tA, __ATOMIC_RELAXED, __HIP_MEMORY_SCOPE_AGENT);
    if constexpr (STAMPS) {
      stamp(18);
      delay(300);
      if (lane < 32) stamps[lane] = lane == 0 ? t_start : *(volatile LAS u64*)(L + L_BYTES + lane * 8);
    }
    return;
  }

  if (wave > NCONS + 1) {
    const int tw = wave - NCONS - 2;
    __builtin_amdgcn_s_barrier();
    wait_ge(W_QPUB, 1u, IC<2>{});
    const unsigned tA = rfl(ctl[W_TAGA]), tB = rfl(ctl[W_TAGB]);
#pragma unroll 1
    for (int br = 0; br < 2; ++br) {
      delay(br ? DQB : QDELAY);
      v2u q[4];
      int off[4];
      const int qb = WS_Q + ((br * 4 + kvh) * 2 + b) * 16384 + tw * 2048 + lane * 16;
#pragma unroll
      for (int j = 0; j < 4; ++j) { off[j] = qb + (j >> 1) * 8192 + (j & 1) * 1024; q[j] = v2u{0u, 0u}; }
      poll(q, off, tA, tB, IC<4>{}, [] {});
      LAS unsigned* qi = (LAS unsigned*)(L + L_QIMG + br * 2048 + tw * 512 + lane * 4);
      qi[0] = cvtpk(f(q[0][0]) + f(q[2][0]), f(q[0][1]) + f(q[2][1]));
      qi[64] = cvtpk(f(q[1][0]) + f(q[3][0]), f(q[1][1]) + f(q[3][1]));
      LDS_WAIT();
      lds_add(W_QRDY + W_PER * br);
      if (tw == 0) stamp(15 + br);
    }
#pragma unroll 1
    for (int br = 0; br < 2; ++br) {
      const int wb = W_PER * br;
      v2u d[9];
      int off[9];
#pragma unroll
      for (int j = 0; j < 8; ++j) { off[j] = WS_H1 + ((br * 32 + (bid & 31)) * 4 + tw) * 8192 + lane * 16 + 1024 * j; d[j] = v2u{0u, 0u}; }
      off[8] = WS_ML + (br * 8 + (tw >> 1) * 4 + ((bid >> 3) & 3)) * 4096 + (4 * (c & 7) + g) * 128 + (bid & 7) * 16;
      d[8] = v2u{0u, 0u};
      if (br == 0) {
        wait_ge(W_MCNT, (unsigned)NCONS, IC<8>{});
        delay(DH1);
        poll(d, off, tA, tB, IC<16>{}, [] {});
      } else {
        wait_ge(W_MCNT + wb, (unsigned)NCONS, IC<2>{});
        hold(D1);
        poll(d, off, tA, tB, IC<0>{}, [] {});
      }
      if (br == 0 && tw == 0) {
        const unsigned mine = rfl(ctl[W_STAMP]);
        float lag = (float)((int)((mine & ~0xfffu) - (d[8][1] & ~0xfffu)) >> 12), dur = (float)(d[8][1] & 0xfffu);
        lag += dpp<ROR1>(lag); lag += dpp<ROR2>(lag); lag += dpp<ROR4>(lag);
        dur += dpp<ROR1>(dur); dur += dpp<ROR2>(dur); dur += dpp<ROR4>(dur);
        const int ticks = (int)f(rfl(u(xrow_sum(lag)))) / 32;
        const float per = f(rfl(u(xrow_sum(dur)))) * (1.0f / 1024.0f);
        const unsigned now = (unsigned)rt();
        const unsigned mean = now - (unsigned)((int)((now << 12) - (mine & ~0xfffu)) >> 12) - (unsigned)ticks;
        const unsigned tau = (unsigned)(per * (0.256f * TPM));
        if (SOFF > -1000) *(volatile LAS v2u*)&ctl[W_BASE] = v2u{mean + (unsigned)SOFF - ((unsigned)(F_VA + NT) * tau >> 8), tau};
        ctl[W_HOLD] = ((mean + (unsigned)((int)(per * (float)(F_KVEND - F_VA - NT)) + HOFF)) << 12) | 1u;
        if constexpr (STAMPS) { if (lane == 0) {
          *(volatile LAS u64*)(L + L_BYTES + 14 * 8) = rt() + (u64)(long long)((int)(ctl[W_HOLD] - ((unsigned)rt() << 12)) >> 12); } }
      }
      float M = f(d[8][0]);
      M = fmaxf(M, dpp<ROR1>(M)); M = fmaxf(M, dpp<ROR2>(M)); M = fmaxf(M, dpp<ROR4>(M));
      M = xrow_max(M);
      const float w = __builtin_amdgcn_exp2f(f(d[8][0]) - M);
      float S = w;
      S += dpp<ROR1>(S); S += dpp<ROR2>(S); S += dpp<ROR4>(S);
      const float inv = 1.0f / xrow_sum(S);
      f32x4 o = zf;
#define ACC(J) o += f32x4{f(d[J][0] << 16), f(d[J][0] & 0xffff0000u), f(d[J][1] << 16), f(d[J][1] & 0xffff0000u)} * dpp<BCAST + J>(w);
      ACC(0) ACC(1) ACC(2) ACC(3) ACC(4) ACC(5) ACC(6) ACC(7)
#undef ACC
      const v2u pk = {cvtpk(xrow_sum(o[0]) * inv, xrow_sum(o[1]) * inv), cvtpk(xrow_sum(o[2]) * inv, xrow_sum(o[3]) * inv)};
      if (lane < 16) *(LAS v2u*)(L + L_OIMG + br * 512 + tw * 128 + lane * 8) = pk;
      LDS_WAIT();
      lds_add(W_ORDY + wb);
      if (tw == 0 && br == 1) stamp(1);
    }
    return;
  }

  const int ci = wave - 1;
  v4u xf[4];
#pragma unroll
  for (int ks = 0; ks < 4; ++ks) xf[ks] = *(const GAS v4u*)(p.x[bid >> 7] + (c & 1) * 1024 + ((bid >> 6) & 1) * 512 + ci * 128 + ks * 32 + 8 * g);
  __builtin_amdgcn_s_barrier();
  vmwait<0>();
  {
    const LAS unsigned char* s = L + L_RING + ci * 2 * SLOT + (8 * g + tq) * 128 + tp * 8;
    v4u w0[8], w1[8];
#pragma unroll
    for (int j = 0; j < 8; ++j) {
      const int o = (j >> 2) * 4096 + (j & 3) * 32;
      w0[j] = trfrag(s + o, s + o + 512); w1[j] = trfrag(s + 8192 + o, s + 8192 + o + 512);
    }
    LDS_WAIT();
    if (lane < 2) ctl[W_FREE + 2 * ci + lane] = (unsigned)(2 * ci + lane + 1);
    PIN8(w0); PIN8(w1);
    f32x4 a[4];
#pragma unroll
    for (int e = 0; e < 4; ++e) a[e] = mfma(w1[4 + e], xf[3], mfma(w1[e], xf[2], mfma(w0[4 + e], xf[1], mfma(w0[e], xf[0], zf))));
    const f32x4 r = pick(a[0], a[1], a[2], a[3]);
    if (c < 8) *(LAS f32x4*)(L + L_QRED + ci * 512 + (c & 1) * 256 + (c >> 1) * 64 + g * 16) = r;
    LDS_WAIT();
    lds_add(W_QCNT);
  }
  wait_ge(W_TAGOK, 1u, IC<0>{});
  const unsigned tA = rfl(ctl[W_TAGA]), tB = rfl(ctl[W_TAGB]);
  if (ci == 0) {
    wait_ge(W_QCNT, (unsigned)NCONS, IC<0>{});
    f32x2 q = {0.f, 0.f};
#pragma unroll
    for (int w = 0; w < NCONS; ++w) q += *(const LAS f32x2*)(L + L_QRED + w * 512 + lane * 8);
    st(v4u{u(q[0]), tA, u(q[1]), tB},
       WS_Q + ((((bid >> 7) * 4 + ((bid >> 4) & 3)) * 2 + (lane >> 5)) * 2 + ((bid >> 6) & 1)) * 8192 + (bid & 15) * 512 + (lane & 31) * 16);
    ctl[W_QPUB] = 1u;
  }
  if (ci == 0) stamp(3);

  v4u keep = zero4, keepl = zero4;
#pragma unroll 1
  for (int br = 0; br < 2; ++br) {
    const int wb = W_PER * br, fk = (br ? F_KB : F_KA) + ci, fv = (br ? F_VB : F_VA) + ci;
    f32x4 sc[TPC][2];
    v4u qf[4];
    unsigned kr = (unsigned)c * 256u + (unsigned)((g ^ c) * 16);
    asm volatile("" : "+v"(kr));
    auto ktile = [&](auto tc) {
      constexpr int t = decltype(tc)::value;
      const int fl = fk + NCONS * t, slot = fl & (NSLOT - 1);
      wait_ge(W_FULL + slot, (unsigned)(fl + 1), IC<1>{});
      v4u K[8];
#pragma unroll
      for (int j = 0; j < 8; ++j) K[j] = *(const LAS v4u*)(L + L_RING + ((kr + slot * SLOT) ^ (64u * (j & 3))) + (j >> 2) * 4096);
      LDS_WAIT();
      ctl[W_FREE + slot] = (unsigned)(fl + 1);
      PIN8(K);
      if constexpr (t == 0) {
        wait_ge(W_QRDY + wb, (unsigned)NCONS, IC<1>{});
#pragma unroll
        for (int j = 0; j < 4; ++j) {
          const v4u q = *(const LAS v4u*)(L + L_QIMG + br * 2048 + (c & 7) * 256 + j * 64 + g * 16);
          qf[j] = c < 8 ? q : zero4;
        }
        if (ci == 0 && br == 0) stamp(4);
      }
      f32x4 s0 = zf, s1 = zf;
#pragma unroll
      for (int j = 0; j < 4; ++j) { s0 = mfma(K[j], qf[j], s0); s1 = mfma(K[4 + j], qf[j], s1); }
      sc[t][0] = s0; sc[t][1] = s1;
    };
    each<0, TPC>(ktile);
    {
      float m = -INFINITY;
#pragma unroll
      for (int t = 0; t < TPC; ++t)
#pragma unroll
        for (int j = 0; j < 2; ++j) m = fmaxf(m, fmaxf(fmaxf(sc[t][j][0], sc[t][j][1]), fmaxf(sc[t][j][2], sc[t][j][3])));
      m = xrow_max(m) * C2;
      if (lane < 8) smax[ci * 8 + lane] = m;
      LDS_WAIT();
      lds_add(W_XCNT + wb);
    }
    wait_ge(W_XCNT + wb, (unsigned)NCONS, IC<0>{});
    v4u pf[TPC];
    {
      const int h = c & 7;
      const float m = fmaxf(fmaxf(smax[h], smax[8 + h]), fmaxf(smax[16 + h], smax[24 + h]));
      float l = 0.f;
#pragma unroll
      for (int t = 0; t < TPC; ++t) {
        float pr[8];
#pragma unroll
        for (int i = 0; i < 8; ++i) pr[i] = __builtin_amdgcn_exp2f(fmaf(sc[t][i >> 2][i & 3], C2, -m));
        pf[t] = v4u{cvtpk(pr[0], pr[1]), cvtpk(pr[2], pr[3]), cvtpk(pr[4], pr[5]), cvtpk(pr[6], pr[7])};
        l += ((pr[0] + pr[1]) + (pr[2] + pr[3])) + ((pr[4] + pr[5]) + (pr[6] + pr[7]));
      }
      l = xrow_sum(l);
      if (lane < 8) ssum[ci * 8 + lane] = l;
      LDS_WAIT();
      lds_add(W_LCNT + wb);
    }
    const int h = 2 * ci + (lane >> 5), l5 = lane & 31;
    wait_ge(W_LCNT + wb, (unsigned)NCONS, IC<0>{});
    const float inv = 1.0f / ((ssum[h] + ssum[8 + h]) + (ssum[16 + h] + ssum[24 + h]));
    const int h8 = lane & 7;
    const float lse = fmaxf(fmaxf(smax[h8], smax[8 + h8]), fmaxf(smax[16 + h8], smax[24 + h8]))
                    + __builtin_amdgcn_logf((ssum[h8] + ssum[8 + h8]) + (ssum[16 + h8] + ssum[24 + h8]));
    f32x4 acc[8];
#pragma unroll
    for (int e = 0; e < 8; ++e) acc[e] = zf;
    unsigned vr = (unsigned)(4 * g + tq) * 256u + (unsigned)((4 * (g & 1) + tq) * 32 + tp * 8);
    asm volatile("" : "+v"(vr));
    auto vtile = [&](auto tc) {
      constexpr int t = decltype(tc)::value;
      const int fl = fv + NCONS * t, slot = fl & (NSLOT - 1);
      wait_ge(W_FULL + slot, (unsigned)(fl + 1), IC<1>{});
      if (t == TPC - 1 && ci == 3 && br == 1) stamp(19);
      v4u V[8];
#pragma unroll
      for (int e = 0; e < 8; ++e) { const LAS unsigned char* s = L + L_RING + ((vr + slot * SLOT) ^ (32u * e)); V[e] = trfrag(s, s + 4096); }
      LDS_WAIT();
      ctl[W_FREE + slot] = (unsigned)(fl + 1);
      PIN8(V);
#pragma unroll
      for (int e = 0; e < 8; ++e) acc[e] = mfma(V[e], pf[t], acc[e]);
    };
    each<0, TPC>(vtile);
    if (c < 8) {
      LAS unsigned char* row = L + L_MRG + (ci * 8 + c) * MROW + g * 16;
#pragma unroll
      for (int e = 0; e < 8; ++e) *(LAS f32x4*)(row + e * 64) = acc[e];
    }
    LDS_WAIT();
    lds_add(W_MCNT + wb);
    const int rec = WS_H1 + ((br * 32 + kvh * 8 + h) * 4 + b * 2 + (l5 >> 4)) * 8192 + part * 256 + (l5 & 15) * 16;
    const int mlo = lane < 8 && ci == 0 ? WS_ML + (br * 8 + sidx) * 4096 + part * 128 + lane * 16 : OOB;
    if (ALATE && br == 1) { st(keep, rec - 32 * 4 * 8192); st(keepl, mlo == OOB ? OOB : mlo - 8 * 4096); }
    wait_ge(W_MCNT + wb, (unsigned)NCONS, IC<0>{});
    const LAS unsigned char* mr = L + L_MRG + h * MROW + l5 * 16;
    const f32x4 o = ((*(const LAS f32x4*)mr + *(const LAS f32x4*)(mr + 8 * MROW)) + (*(const LAS f32x4*)(mr + 16 * MROW) + *(const LAS f32x4*)(mr + 24 * MROW))) * inv;
    keep = v4u{cvtpk(o[0], o[1]), tA, cvtpk(o[2], o[3]), tB};
    keepl = v4u{u(lse), tA, ctl[W_STAMP], tB};
    if (br == 1) hold(0);
    if (!ALATE || br == 1) { st(keep, rec); st(keepl, mlo); }
    if (ci == 3) stamp(br ? 6 : 5);
  }
#pragma unroll 1
  for (int br = 0; br < 2; ++br) {
    spin([&] { return wo_ready(F_WOA + 4 * br); }, IC<1>{});
    v4u wf[8];
    wo_frags(wf, F_WOA + 4 * br, ci);
    project(wf, br, ci, tA, tB);
    if (ci == 0) stamp(br ? 7 : 22);
  }
}

template <int SOFF, int HOFF, int WOL, int D1, int D2, int STAMPS>
static void launch(void* const* d_in, void* d_out, void* d_ws, hipStream_t stream) {
  Params p;
  for (int br = 0; br < 2; ++br) {
    p.x[br] = (const u16*)d_in[5 * br + 0]; p.k[br] = (const u16*)d_in[5 * br + 1]; p.v[br] = (const u16*)d_in[5 * br + 2];
    p.wq[br] = (const u16*)d_in[5 * br + 3]; p.wo[br] = (const u16*)d_in[5 * br + 4];
  }
  p.out = (u16*)d_out; p.ws = (unsigned char*)d_ws;
  constexpr int bytes = L_BYTES + (STAMPS ? 256 : 0);
  (void)hipFuncSetAttribute((const void*)k_fused<SOFF, HOFF, WOL, D1, D2, STAMPS>, hipFuncAttributeMaxDynamicSharedMemorySize, bytes);
  hipLaunchKernelGGL((k_fused<SOFF, HOFF, WOL, D1, D2, STAMPS>), dim3(NWG), dim3(64 * NWAVE), bytes, stream, p);
}

}

extern "C" void kernel_launch(void* const* d_in, const int* in_sizes, int n_in, void* d_out, int out_size, void* d_ws, size_t ws_size,
                              hipStream_t stream) {
  fused::launch<CFG>(d_in, d_out, d_ws, stream);
}
```

```cpp
#include <hip/hip_runtime.h>
#include <hip/hip_bf16.h>
#include <cstdint>

#define GAS __attribute__((address_space(1)))
#define LAS __attribute__((address_space(3)))
typedef unsigned v4u __attribute__((ext_vector_type(4)));
typedef unsigned v2u __attribute__((ext_vector_type(2)));
typedef float f32x4 __attribute__((ext_vector_type(4)));
typedef float f32x2 __attribute__((ext_vector_type(2)));
typedef short bf16x8 __attribute__((ext_vector_type(8)));
typedef short s16x4 __attribute__((ext_vector_type(4)));
typedef unsigned short u16;
typedef unsigned long long u64;

#define CFG 70, 1000, 170, 20, 30, 0

namespace fused {

constexpr int SK = 32768, DH = 128;
constexpr int NWG = 256, NCONS = 4, NWAVE = 10;
constexpr int NSLOT = 16, SLOT = 8192;
constexpr int NT = 32, TPC = NT / NCONS;
constexpr int F_KA = 8, F_VA = F_KA + NT;
constexpr float C2 = 0.088388347648318440f * 1.4426950408889634f;
constexpr u64 TCAP = 2000000;
constexpr int OOB = 0x7ffffff0;

constexpr int WS_EPOCH = 0;
constexpr int WS_ML = 4096;
constexpr int WS_Q = WS_ML + 2 * 8 * 4096;
constexpr int WS_H2 = WS_Q + 2 * 8 * 16384;
constexpr int WS_H1 = WS_H2 + 256 * 4096;
constexpr int WS_END = WS_H1 + 2 * 128 * 8192;
constexpr int WS_STAMP = WS_END;

constexpr int L_RING = 0;
constexpr int L_CTL = NSLOT * SLOT;
constexpr int L_QIMG = L_CTL + 512;
constexpr int L_QRED = L_QIMG + 4096;
constexpr int L_OIMG = L_QRED + 2048;
constexpr int L_STAT = L_OIMG + 1024;
constexpr int MROW = 528;
constexpr int L_MRG = L_STAT + 512;
constexpr int L_BYTES = L_MRG + 4 * 8 * MROW;
enum { W_FULL = 0, W_FREE = 16, W_ABORT = 32, W_TAGA, W_TAGB, W_TAGOK, W_STAMP, W_HOLD, W_BASE, W_TAU,
       W_QCNT = 40, W_QPUB, W_QRDY, W_XCNT, W_LCNT, W_SRDY, W_MCNT, W_ORDY, W_WCNT, W_PER = 16 };
static_assert(L_BYTES <= 160 * 1024 && L_BYTES > 80 * 1024, "one workgroup per CU");

struct Params { const u16* x[2]; const u16* k[2]; const u16* v[2]; const u16* wq[2]; const u16* wo[2]; u16* out; unsigned char* ws; };

__device__ __forceinline__ u64 rt() { return __builtin_amdgcn_s_memrealtime(); }
__device__ __forceinline__ unsigned rfl(unsigned v) { return (unsigned)__builtin_amdgcn_readfirstlane((int)v); }
__device__ __forceinline__ unsigned cvtpk(float lo, float hi) {
  unsigned r; asm("v_cvt_pk_bf16_f32 %0, %1, %2" : "=v"(r) : "v"(lo), "v"(hi)); return r;
}
__device__ __forceinline__ f32x4 mfma(v4u a, v4u b, f32x4 c) {
  return __builtin_amdgcn_mfma_f32_16x16x32_bf16(__builtin_bit_cast(bf16x8, a), __builtin_bit_cast(bf16x8, b), c, 0, 0, 0);
}
__device__ __forceinline__ float f(unsigned u) { return __uint_as_float(u); }
__device__ __forceinline__ unsigned u(float x) { return __float_as_uint(x); }
__device__ __forceinline__ float xrow_max(float x) {
  auto s = __builtin_amdgcn_permlane16_swap(u(x), u(x), false, false);
  x = fmaxf(f(s[0]), f(s[1]));
  auto t = __builtin_amdgcn_permlane32_swap(u(x), u(x), false, false);
  return fmaxf(f(t[0]), f(t[1]));
}
__device__ __forceinline__ float xrow_sum(float x) {
  auto s = __builtin_amdgcn_permlane16_swap(u(x), u(x), false, false);
  x = f(s[0]) + f(s[1]);
  auto t = __builtin_amdgcn_permlane32_swap(u(x), u(x), false, false);
  return f(t[0]) + f(t[1]);
}
template <int CTRL> __device__ __forceinline__ float dpp(float x) {
  return __builtin_bit_cast(float, __builtin_amdgcn_mov_dpp(__builtin_bit_cast(int, x), CTRL, 0xf, 0xf, true));
}
constexpr int ROR1 = 0x121, ROR2 = 0x122, ROR4 = 0x124, ROR8 = 0x128, BCAST = 0x150;
template <int N> struct IC { static constexpr int value = N; };
template <int I, int E, class F> __device__ __forceinline__ void each(F& fn) {
  if constexpr (I < E) { fn(IC<I>{}); each<I + 1, E>(fn); }
}
template <int N> __device__ __forceinline__ void vmwait() { asm volatile("s_waitcnt vmcnt(%0)" ::"i"(N) : "memory"); }
#define LDS_WAIT() asm volatile("s_waitcnt lgkmcnt(0)" ::: "memory")
#define LDS_ACQ() __builtin_amdgcn_fence(__ATOMIC_ACQUIRE, "workgroup")
#define SCHED() __builtin_amdgcn_sched_barrier(0)
#define PIN8(w) asm volatile("" : "+v"(w[0]), "+v"(w[1]), "+v"(w[2]), "+v"(w[3]), "+v"(w[4]), "+v"(w[5]), "+v"(w[6]), "+v"(w[7]) : : "memory")
#define GLDS(src, dst, imm) __builtin_amdgcn_global_load_lds((const GAS unsigned*)(src), (LAS unsigned*)(dst), 16, (imm), 2)

__device__ __forceinline__ v4u trfrag(const LAS unsigned char* lo, const LAS unsigned char* hi) {
  const v2u a = __builtin_bit_cast(v2u, __builtin_amdgcn_ds_read_tr16_b64_v4i16((LAS s16x4*)lo));
  const v2u b = __builtin_bit_cast(v2u, __builtin_amdgcn_ds_read_tr16_b64_v4i16((LAS s16x4*)hi));
  return v4u{a[0], a[1], b[0], b[1]};
}

template <int SOFF, int TPM, int HREL, int BEH, int LEAD, int STAMPS>
__global__ void __launch_bounds__(64 * NWAVE) k_fused(Params p) {
  constexpr int DQB = 600, QDELAY = 100, HOFF = SOFF + HREL, RT = 1, STAG = 1, NAPC = 4, WN = 16, WOL = 1, D1 = 70, D2 = 60, HALF = 0, ALATE = 0, DH1 = 250, NARROW = 0, HELD = 0, WX = 16;
  static_assert(WOL == 1, "");
  constexpr int F_KB = F_VA + NT + (WOL ? 0 : 4), F_VB = F_KB + NT, F_KVEND = F_VB + NT, F_WOA = WOL ? F_KVEND : F_VA + NT, F_WOB = F_KVEND + (WOL ? 4 : 0);
  static_assert(WN % 4 == 0 && (HALF || WN % 8 == 0) && WN >= 8 && WN <= 48 && RT >= 0 && RT <= 5, "pieces left in flight; run of 2^RT tiles");
  extern __shared__ __attribute__((aligned(16))) unsigned char lds_raw[];
  LAS unsigned char* L = (LAS unsigned char*)lds_raw;
  volatile LAS unsigned* ctl = (volatile LAS unsigned*)(L + L_CTL);
  const u64 t_start = rt();
  const int bid = blockIdx.x, sidx = bid >> 5, part = bid & 31, b = sidx >> 2, kvh = sidx & 3;
  const int tid = threadIdx.x, lane = tid & 63, wave = (int)rfl(tid >> 6);
  const int c = lane & 15, g = lane >> 4;
  if (__builtin_expect(wave >= 1 && wave <= NCONS, 1)) {
    const unsigned char* wq = (const unsigned char*)p.wq[bid >> 7] + (size_t)(((bid >> 6) & 1) * 512 + (wave - 1) * 128) * 8192 + (bid & 63) * 128;
    const unsigned wq_off = (unsigned)(lane >> 3) * 8192u + (unsigned)(lane & 7) * 16u;
#pragma unroll
    for (int j = 0; j < 16; ++j) GLDS(wq + (size_t)j * 8 * 8192 + wq_off, L + L_RING + (wave - 1) * 2 * SLOT + j * 1024, 0);
  }
  u64* stamps = (u64*)(p.ws + WS_STAMP) + bid * 32;
  auto stamp = [&](int i) { if constexpr (STAMPS) { const u64 t = rt(); if (lane == 0) *(volatile LAS u64*)(L + L_BYTES + i * 8) = t; } };

  auto spin = [&](auto&& cond, auto nap) {
    for (unsigned it = 1; !cond(); ++it) {
      if constexpr (decltype(nap)::value > 0) __builtin_amdgcn_s_sleep(decltype(nap)::value);
      if ((it & 63u) == 0u) {
        if (rt() - t_start > TCAP) ctl[W_ABORT] = 1u;
        if (rfl(ctl[W_ABORT])) break;
      }
    }
  };
  auto wait_ge = [&](int w, unsigned n, auto nap) {
    spin([&] { return rfl(ctl[w]) >= n; }, nap);
    LDS_ACQ();
  };

  if (__builtin_expect(wave == 0, 1)) {
    __builtin_amdgcn_s_setprio(3);
    ctl[lane] = 0u; ctl[64 + lane] = 0u;
    unsigned koff[4], voff[2];
#pragma unroll
    for (int j = 0; j < 4; ++j) koff[j] = (unsigned)g * 256u + (unsigned)((c ^ (4 * j + g)) * 16);
#pragma unroll
    for (int j = 0; j < 2; ++j) voff[j] = (unsigned)g * 256u + (unsigned)((c ^ (2 * (4 * j + g))) * 16);
    constexpr int LAG = WN / 8 + 1;
    int fill = F_KA, lastpark = 0;
    u64 parked = 0;
    unsigned fr = 0u;
    auto full = [&](int fl) { ctl[W_FULL + (fl & (NSLOT - 1))] = (unsigned)(fl + 1); };
    auto drain = [&]() {
      static_assert(LAG + WX / 8 <= 6, "");
      vmwait<40>(); full(fill - 6); vmwait<32>(); full(fill - 5); vmwait<24>(); full(fill - 4);
      vmwait<16>(); full(fill - 3); vmwait<8>(); full(fill - 2); vmwait<0>(); full(fill - 1);
    };
    auto room = [&]() {
      if (rfl(fr) < (unsigned)(fill - NSLOT + 1) && fill >= NSLOT) {
        const u64 tp = rt();
        drain();
        spin([&] { return rfl(ctl[W_FREE + (fill & (NSLOT - 1))]) >= (unsigned)(fill - NSLOT + 1); }, IC<1>{});
        parked += rt() - tp; lastpark = fill;
      }
    };
    v2u sched = {0u, 0u};
    unsigned now = 0u, nwide = 0u;
    auto issue = [&](const unsigned char* s0, const unsigned char* s1, unsigned o0, unsigned o1, unsigned o2, unsigned o3) {
      const unsigned dst = (unsigned)(size_t)(L + L_RING) + (unsigned)((fill & (NSLOT - 1)) * SLOT);
      room();
      bool wide = false;
      const unsigned tau = rfl(sched[1]);
      if (tau != 0u) {
        const unsigned due = rfl(sched[0]) + ((unsigned)fill * tau >> 8);
        int late = (int)(now - due);
        wide = late > BEH && fill < F_KVEND - NARROW;
        while (late < -LEAD && late > -1000) { __builtin_amdgcn_s_sleep(1); late = (int)((unsigned)rt() - due); }
      }
#define FILL(N) asm volatile("s_mov_b32 m0, %0\n\ts_waitcnt vmcnt(%7)\n\t"                                                     \
                   "global_load_lds_dwordx4 %3, %1 nt\n\tglobal_load_lds_dwordx4 %4, %1 offset:1024 nt\n\t"                    \
                   "global_load_lds_dwordx4 %5, %1 offset:2048 nt\n\tglobal_load_lds_dwordx4 %6, %1 offset:3072 nt\n\t"        \
                   "s_add_u32 m0, %0, 0x1000\n\ts_nop 0\n\t"                                                                  \
                   "global_load_lds_dwordx4 %3, %2 nt\n\tglobal_load_lds_dwordx4 %4, %2 offset:1024 nt\n\t"                    \
                   "global_load_lds_dwordx4 %5, %2 offset:2048 nt\n\tglobal_load_lds_dwordx4 %6, %2 offset:3072 nt"            \
                   :: "s"(dst), "s"(s0), "s"(s1), "v"(o0), "v"(o1), "v"(o2), "v"(o3), "i"(N) : "memory", "scc")
      if (wide) {
        ++nwide;
        FILL(WN + WX);
        full(fill - LAG - WX / 8);
      } else {
        FILL(WN);
#pragma unroll
        for (int j = WX / 8; j >= 0; --j) full(fill - LAG - j);
      }
#undef FILL
      ++fill;
      fr = ctl[W_FREE + (fill & (NSLOT - 1))];
      sched = *(volatile LAS v2u*)&ctl[W_BASE];
      now = (unsigned)rt();
    };
    const size_t sbase = (size_t)sidx * SK * DH * 2;
    auto tile_off = [&](int i) -> size_t {
      constexpr int NR = NT >> RT;
      return (size_t)((((((i >> RT) + STAG * sidx) & (NR - 1)) * 32 + part) << RT) | (i & ((1 << RT) - 1))) * 8192;
    };
    const size_t wo_base = (size_t)((bid & 31) * 128) * 2048 + (bid >> 5) * 256;
    const unsigned woff = (unsigned)g * 2048u + (unsigned)c * 16u;
    LDS_WAIT();
    __builtin_amdgcn_s_barrier();
    stamp(11);
#pragma unroll 1
    for (int br = 0; br < 2; ++br) {
      const unsigned char* kb = (const unsigned char*)p.k[br] + sbase;
      const unsigned char* vb = (const unsigned char*)p.v[br] + sbase;
#pragma unroll 1
      for (int i = 0; i < NT; ++i) { const unsigned char* s = kb + tile_off(i); issue(s, s + 4096, koff[0], koff[1], koff[2], koff[3]); }
      stamp(24 + 2 * br);
      const unsigned tk = (unsigned)rt();
#pragma unroll 1
      for (int i = 0; i < NT; ++i) { const unsigned char* s = vb + tile_off(i); issue(s, s + 4096, voff[0], voff[1], voff[0], voff[1]); }
      stamp(25 + 2 * br);
      if (br == 0) { const unsigned tv = (unsigned)rt(); ctl[W_STAMP] = (tv << 12) | ((tv - tk) & 0xfffu); }
#pragma unroll 1
      for (int i = 0; i < (WOL ? 8 * br : 4); ++i) {
        const unsigned char* s = (const unsigned char*)p.wo[WOL ? i >> 2 : br] + wo_base + (size_t)((i & 3) * 32) * 2048;
        issue(s, s + 4 * 8192, woff, woff + 7168u, woff + 14336u, woff + 21504u);
      }
    }
    drain();
    stamp(9);
    if constexpr (STAMPS) { if (lane == 0) *(volatile LAS u64*)(L + L_BYTES + 23 * 8) = (u64)nwide * 100; }
    if constexpr (STAMPS) { if (lane == 0) { *(volatile LAS u64*)(L + L_BYTES + 20 * 8) = parked; *(volatile LAS u64*)(L + L_BYTES + 21 * 8) = (u64)lastpark * 100; } }
    return;
  }

  const __amdgpu_buffer_rsrc_t rs = __builtin_amdgcn_make_buffer_rsrc(p.ws, 0, WS_END, 0x00020000);
  auto ld = [&](int off) -> v4u { return __builtin_amdgcn_raw_buffer_load_b128(rs, off, 0, 16); };
  auto st = [&](v4u v, int off) { __builtin_amdgcn_raw_buffer_store_b128(v, rs, off, 0, 16); };
  auto lds_add = [&](int w) { if (lane == 0) __hip_atomic_fetch_add((LAS unsigned*)&ctl[w], 1u, __ATOMIC_RELAXED, __HIP_MEMORY_SCOPE_WORKGROUP); };
  auto delay = [&](u64 ticks) { const u64 t0 = rt(); while (rt() - t0 < ticks) __builtin_amdgcn_s_sleep(2); };
  auto hold = [&](int extra) {
    const unsigned h = rfl(ctl[W_HOLD]);
    if (HOFF > -1000 && h != 0u) {
      for (;;) {
        const int left = ((int)(h - ((unsigned)rt() << 12)) >> 12) + extra;
        if (left <= 0 || left > 800) break;
        __builtin_amdgcn_s_sleep(1);
      }
    }
  };
  auto poll = [&](auto& v, auto& off, unsigned tA, unsigned tB, auto nap, auto&& shadow) {
    constexpr int N = sizeof(v) / sizeof(v[0]);
    spin([&] {
      v4u t[N];
#pragma unroll
      for (int j = 0; j < N; ++j) t[j] = ld(off[j]);
      SCHED();
      shadow();
      SCHED();
      bool pend = false;
#pragma unroll
      for (int j = 0; j < N; ++j) {
        const bool hit = (t[j][1] == tA) & (t[j][3] == tB) & (off[j] != OOB);
        v[j][0] = hit ? t[j][0] : v[j][0];
        v[j][1] = hit ? t[j][2] : v[j][1];
        off[j] = hit ? OOB : off[j];
        pend = pend | (off[j] != OOB);
      }
      return __any(pend) == 0;
    }, nap);
  };
  auto gather = [&](auto& v, int base, unsigned tA, unsigned tB, auto nap, auto&& shadow) {
    constexpr int N = sizeof(v) / sizeof(v[0]);
    int off[N];
#pragma unroll
    for (int j = 0; j < N; ++j) { off[j] = base + lane * 16 + 1024 * j; v[j] = v2u{0u, 0u}; }
    poll(v, off, tA, tB, nap, shadow);
  };
  const int tq = (lane >> 2) & 3, tp = lane & 3;
  const v4u zero4 = {0u, 0u, 0u, 0u};
  const f32x4 zf = {0.f, 0.f, 0.f, 0.f};
  auto pick = [&](f32x4 a0, f32x4 a1, f32x4 a2, f32x4 a3) -> f32x4 {
    const f32x4 lo = (c & 2) ? a1 : a0, hi = (c & 2) ? a3 : a2;
    return (c & 4) ? hi : lo;
  };
  LAS float* smax = (LAS float*)(L + L_STAT);
  LAS float* ssum = smax + 32;

  auto wo_ready = [&](int f0) { return rfl(ctl[W_FULL + ((f0 + 3) & (NSLOT - 1))]) >= (unsigned)(f0 + 4); };
  auto wo_frags = [&](v4u (&wf)[8], int f0, int w) {
    LDS_ACQ();
    const LAS unsigned char* s = L + L_RING + (f0 & (NSLOT - 1)) * SLOT + (8 * g + tq) * 256 + w * 64 + tp * 8;
#pragma unroll
    for (int j = 0; j < 8; ++j) wf[j] = trfrag(s + (j >> 1) * 8192 + (j & 1) * 32, s + (j >> 1) * 8192 + (j & 1) * 32 + 1024);
  };
  auto project = [&](const v4u (&wf)[8], int br, int w, unsigned tA, unsigned tB) {
    const int o2 = c < 8 ? WS_H2 + (br * 128 + ((bid >> 5) * 4 + w) * 4 + g) * 4096 + (bid & 31) * 128 + c * 16 : OOB;
    wait_ge(W_ORDY + W_PER * br, (unsigned)NCONS, IC<0>{});
    f32x4 a0 = zf, a1 = zf;
#pragma unroll
    for (int ks = 0; ks < 4; ++ks) {
      const v4u of = *(const LAS v4u*)(L + L_OIMG + br * 512 + (c & 1) * 256 + ks * 64 + g * 16);
      a0 = mfma(wf[2 * ks], of, a0);
      a1 = mfma(wf[2 * ks + 1], of, a1);
    }
    const f32x4 r = (c & 2) ? a1 : a0;
    st(v4u{u((c & 4) ? r[2] : r[0]), tA, u((c & 4) ? r[3] : r[1]), tB}, o2);
  };

  if (wave == NCONS + 1) {
    const u64 ep = __hip_atomic_load((GAS u64*)(p.ws + WS_EPOCH), __ATOMIC_RELAXED, __HIP_MEMORY_SCOPE_AGENT);
    __builtin_amdgcn_s_barrier();
    const unsigned tA = rfl((unsigned)ep) + 1u, tB = rfl((unsigned)(ep >> 32)) + 1u;
    ctl[W_TAGA] = tA; ctl[W_TAGB] = tB;
    LDS_WAIT();
    ctl[W_TAGOK] = 1u;
    auto reduce = [&](auto nap) {
      v2u r[4];
      gather(r, WS_H2 + ((bid & 1) * 128 + (bid >> 1)) * 4096, tA, tB, nap, [] {});
      float s0 = (f(r[0][0]) + f(r[1][0])) + (f(r[2][0]) + f(r[3][0])), s1 = (f(r[0][1]) + f(r[1][1])) + (f(r[2][1]) + f(r[3][1]));
      s0 += dpp<ROR8>(s0); s1 += dpp<ROR8>(s1);
      s0 = xrow_sum(s0); s1 = xrow_sum(s1);
      if (lane < 8) {
        const int col = 32 * (bid >> 3) + 16 * ((lane >> 1) & 1) + 4 * ((bid >> 1) & 3) + 2 * (lane >> 2);
        *(GAS unsigned*)(p.out + (lane & 1) * 2048 + (bid & 1) * 1024 + col) = cvtpk(s0, s1);
      }
    };
    wait_ge(W_ORDY + W_PER, (unsigned)NCONS, IC<2>{});
    stamp(17);
    if (bid & 1) delay(D2);
    reduce(IC<0>{});
    if (bid == 1 && lane == 0)
      __hip_atomic_store((GAS u64*)(p.ws + WS_EPOCH), ((u64)tB << 32) | tA, __ATOMIC_RELAXED, __HIP_MEMORY_SCOPE_AGENT);
    if constexpr (STAMPS) {
      stamp(18);
      delay(300);
      if (lane < 32) stamps[lane] = lane == 0 ? t_start : *(volatile LAS u64*)(L + L_BYTES + lane * 8);
    }
    return;
  }

  if (wave > NCONS + 1) {
    const int tw = wave - NCONS - 2;
    __builtin_amdgcn_s_barrier();
    wait_ge(W_QPUB, 1u, IC<2>{});
    const unsigned tA = rfl(ctl[W_TAGA]), tB = rfl(ctl[W_TAGB]);
#pragma unroll 1
    for (int br = 0; br < 2; ++br) {
      delay(br ? DQB : QDELAY);
      v2u q[4];
      int off[4];
      const int qb = WS_Q + ((br * 4 + kvh) * 2 + b) * 16384 + tw * 2048 + lane * 16;
#pragma unroll
      for (int j = 0; j < 4; ++j) { off[j] = qb + (j >> 1) * 8192 + (j & 1) * 1024; q[j] = v2u{0u, 0u}; }
      poll(q, off, tA, tB, IC<4>{}, [] {});
      LAS unsigned* qi = (LAS unsigned*)(L + L_QIMG + br * 2048 + tw * 512 + lane * 4);
      qi[0] = cvtpk(f(q[0][0]) + f(q[2][0]), f(q[0][1]) + f(q[2][1]));
      qi[64] = cvtpk(f(q[1][0]) + f(q[3][0]), f(q[1][1]) + f(q[3][1]));
      LDS_WAIT();
      lds_add(W_QRDY + W_PER * br);
      if (tw == 0) stamp(15 + br);
    }
#pragma unroll 1
    for (int br = 0; br < 2; ++br) {
      const int wb = W_PER * br;
      v2u d[9];
      int off[9];
#pragma unroll
      for (int j = 0; j < 8; ++j) { off[j] = WS_H1 + ((br * 32 + (bid & 31)) * 4 + tw) * 8192 + lane * 16 + 1024 * j; d[j] = v2u{0u, 0u}; }
      off[8] = WS_ML + (br * 8 + (tw >> 1) * 4 + ((bid >> 3) & 3)) * 4096 + (4 * (c & 7) + g) * 128 + (bid & 7) * 16;
      d[8] = v2u{0u, 0u};
      if (br == 0) {
        wait_ge(W_MCNT, (unsigned)NCONS, IC<8>{});
        delay(DH1);
        poll(d, off, tA, tB, IC<16>{}, [] {});
      } else {
        wait_ge(W_MCNT + wb, (unsigned)NCONS, IC<2>{});
        hold(D1);
        poll(d, off, tA, tB, IC<0>{}, [] {});
      }
      if (br == 0 && tw == 0) {
        const unsigned mine = rfl(ctl[W_STAMP]);
        float lag = (float)((int)((mine & ~0xfffu) - (d[8][1] & ~0xfffu)) >> 12), dur = (float)(d[8][1] & 0xfffu);
        lag += dpp<ROR1>(lag); lag += dpp<ROR2>(lag); lag += dpp<ROR4>(lag);
        dur += dpp<ROR1>(dur); dur += dpp<ROR2>(dur); dur += dpp<ROR4>(dur);
        const int ticks = (int)f(rfl(u(xrow_sum(lag)))) / 32;
        const float per = f(rfl(u(xrow_sum(dur)))) * (1.0f / 1024.0f);
        const unsigned now = (unsigned)rt();
        const unsigned mean = now - (unsigned)((int)((now << 12) - (mine & ~0xfffu)) >> 12) - (unsigned)ticks;
        const unsigned tau = (unsigned)(per * (0.256f * TPM));
        if (SOFF > -1000) *(volatile LAS v2u*)&ctl[W_BASE] = v2u{mean + (unsigned)SOFF - ((unsigned)(F_VA + NT) * tau >> 8), tau};
        ctl[W_HOLD] = ((mean + (unsigned)((int)(per * (0.001f * TPM * (F_KVEND - F_VA - NT))) + HOFF)) << 12) | 1u;
        if constexpr (STAMPS) { if (lane == 0) {
          *(volatile LAS u64*)(L + L_BYTES + 14 * 8) = rt() + (u64)(long long)((int)(ctl[W_HOLD] - ((unsigned)rt() << 12)) >> 12); } }
      }
      float M = f(d[8][0]);
      M = fmaxf(M, dpp<ROR1>(M)); M = fmaxf(M, dpp<ROR2>(M)); M = fmaxf(M, dpp<ROR4>(M));
      M = xrow_max(M);
      const float w = __builtin_amdgcn_exp2f(f(d[8][0]) - M);
      float S = w;
      S += dpp<ROR1>(S); S += dpp<ROR2>(S); S += dpp<ROR4>(S);
      const float inv = 1.0f / xrow_sum(S);
      f32x4 o = zf;
#define ACC(J) o += f32x4{f(d[J][0] << 16), f(d[J][0] & 0xffff0000u), f(d[J][1] << 16), f(d[J][1] & 0xffff0000u)} * dpp<BCAST + J>(w);
      ACC(0) ACC(1) ACC(2) ACC(3) ACC(4) ACC(5) ACC(6) ACC(7)
#undef ACC
      const v2u pk = {cvtpk(xrow_sum(o[0]) * inv, xrow_sum(o[1]) * inv), cvtpk(xrow_sum(o[2]) * inv, xrow_sum(o[3]) * inv)};
      if (lane < 16) *(LAS v2u*)(L + L_OIMG + br * 512 + tw * 128 + lane * 8) = pk;
      LDS_WAIT();
      lds_add(W_ORDY + wb);
      if (tw == 0 && br == 1) stamp(1);
    }
    return;
  }

  const int ci = wave - 1;
  v4u xf[4];
#pragma unroll
  for (int ks = 0; ks < 4; ++ks) xf[ks] = *(const GAS v4u*)(p.x[bid >> 7] + (c & 1) * 1024 + ((bid >> 6) & 1) * 512 + ci * 128 + ks * 32 + 8 * g);
  __builtin_amdgcn_s_barrier();
  vmwait<0>();
  {
    const LAS unsigned char* s = L + L_RING + ci * 2 * SLOT + (8 * g + tq) * 128 + tp * 8;
    v4u w0[8], w1[8];
#pragma unroll
    for (int j = 0; j < 8; ++j) {
      const int o = (j >> 2) * 4096 + (j & 3) * 32;
      w0[j] = trfrag(s + o, s + o + 512); w1[j] = trfrag(s + 8192 + o, s + 8192 + o + 512);
    }
    LDS_WAIT();
    if (lane < 2) ctl[W_FREE + 2 * ci + lane] = (unsigned)(2 * ci + lane + 1);
    PIN8(w0); PIN8(w1);
    f32x4 a[4];
#pragma unroll
    for (int e = 0; e < 4; ++e) a[e] = mfma(w1[4 + e], xf[3], mfma(w1[e], xf[2], mfma(w0[4 + e], xf[1], mfma(w0[e], xf[0], zf))));
    const f32x4 r = pick(a[0], a[1], a[2], a[3]);
    if (c < 8) *(LAS f32x4*)(L + L_QRED + ci * 512 + (c & 1) * 256 + (c >> 1) * 64 + g * 16) = r;
    LDS_WAIT();
    lds_add(W_QCNT);
  }
  wait_ge(W_TAGOK, 1u, IC<0>{});
  const unsigned tA = rfl(ctl[W_TAGA]), tB = rfl(ctl[W_TAGB]);
  if (ci == 0) {
    wait_ge(W_QCNT, (unsigned)NCONS, IC<0>{});
    f32x2 q = {0.f, 0.f};
#pragma unroll
    for (int w = 0; w < NCONS; ++w) q += *(const LAS f32x2*)(L + L_QRED + w * 512 + lane * 8);
    st(v4u{u(q[0]), tA, u(q[1]), tB},
       WS_Q + ((((bid >> 7) * 4 + ((bid >> 4) & 3)) * 2 + (lane >> 5)) * 2 + ((bid >> 6) & 1)) * 8192 + (bid & 15) * 512 + (lane & 31) * 16);
    ctl[W_QPUB] = 1u;
  }
  if (ci == 0) stamp(3);

  v4u keep = zero4, keepl = zero4;
#pragma unroll 1
  for (int br = 0; br < 2; ++br) {
    const int wb = W_PER * br, fk = (br ? F_KB : F_KA) + ci, fv = (br ? F_VB : F_VA) + ci;
    f32x4 sc[TPC][2];
    v4u qf[4];
    unsigned kr = (unsigned)c * 256u + (unsigned)((g ^ c) * 16);
    asm volatile("" : "+v"(kr));
    auto ktile = [&](auto tc) {
      constexpr int t = decltype(tc)::value;
      const int fl = fk + NCONS * t, slot = fl & (NSLOT - 1);
      wait_ge(W_FULL + slot, (unsigned)(fl + 1), IC<NAPC>{});
      v4u K[8];
#pragma unroll
      for (int j = 0; j < 8; ++j) K[j] = *(const LAS v4u*)(L + L_RING + ((kr + slot * SLOT) ^ (64u * (j & 3))) + (j >> 2) * 4096);
      LDS_WAIT();
      ctl[W_FREE + slot] = (unsigned)(fl + 1);
      PIN8(K);
      if constexpr (t == 0) {
        wait_ge(W_QRDY + wb, (unsigned)NCONS, IC<1>{});
#pragma unroll
        for (int j = 0; j < 4; ++j) {
          const v4u q = *(const LAS v4u*)(L + L_QIMG + br * 2048 + (c & 7) * 256 + j * 64 + g * 16);
          qf[j] = c < 8 ? q : zero4;
        }
        if (ci == 0 && br == 0) stamp(4);
      }
      f32x4 s0 = zf, s1 = zf;
#pragma unroll
      for (int j = 0; j < 4; ++j) { s0 = mfma(K[j], qf[j], s0); s1 = mfma(K[4 + j], qf[j], s1); }
      sc[t][0] = s0; sc[t][1] = s1;
    };
    each<0, TPC>(ktile);
    {
      float m = -INFINITY;
#pragma unroll
      for (int t = 0; t < TPC; ++t)
#pragma unroll
        for (int j = 0; j < 2; ++j) m = fmaxf(m, fmaxf(fmaxf(sc[t][j][0], sc[t][j][1]), fmaxf(sc[t][j][2], sc[t][j][3])));
      m = xrow_max(m) * C2;
      if (lane < 8) smax[ci * 8 + lane] = m;
      LDS_WAIT();
      lds_add(W_XCNT + wb);
    }
    wait_ge(W_XCNT + wb, (unsigned)NCONS, IC<0>{});
    v4u pf[TPC];
    {
      const int h = c & 7;
      const float m = fmaxf(fmaxf(smax[h], smax[8 + h]), fmaxf(smax[16 + h], smax[24 + h]));
      float l = 0.f;
#pragma unroll
      for (int t = 0; t < TPC; ++t) {
        float pr[8];
#pragma unroll
        for (int i = 0; i < 8; ++i) pr[i] = __builtin_amdgcn_exp2f(fmaf(sc[t][i >> 2][i & 3], C2, -m));
        pf[t] = v4u{cvtpk(pr[0], pr[1]), cvtpk(pr[2], pr[3]), cvtpk(pr[4], pr[5]), cvtpk(pr[6], pr[7])};
        l += ((pr[0] + pr[1]) + (pr[2] + pr[3])) + ((pr[4] + pr[5]) + (pr[6] + pr[7]));
      }
      l = xrow_sum(l);
      if (lane < 8) ssum[ci * 8 + lane] = l;
      LDS_WAIT();
      lds_add(W_LCNT + wb);
    }
    const int h = 2 * ci + (lane >> 5), l5 = lane & 31;
    wait_ge(W_LCNT + wb, (unsigned)NCONS, IC<0>{});
    const float inv = 1.0f / ((ssum[h] + ssum[8 + h]) + (ssum[16 + h] + ssum[24 + h]));
    const int h8 = lane & 7;
    const float lse = fmaxf(fmaxf(smax[h8], smax[8 + h8]), fmaxf(smax[16 + h8], smax[24 + h8]))
                    + __builtin_amdgcn_logf((ssum[h8] + ssum[8 + h8]) + (ssum[16 + h8] + ssum[24 + h8]));
    f32x4 acc[8];
#pragma unroll
    for (int e = 0; e < 8; ++e) acc[e] = zf;
    unsigned vr = (unsigned)(4 * g + tq) * 256u + (unsigned)((4 * (g & 1) + tq) * 32 + tp * 8);
    asm volatile("" : "+v"(vr));
    auto vtile = [&](auto tc) {
      constexpr int t = decltype(tc)::value;
      const int fl = fv + NCONS * t, slot = fl & (NSLOT - 1);
      wait_ge(W_FULL + slot, (unsigned)(fl + 1), IC<(t == TPC - 1 ? 1 : NAPC)>{});
      if (t == TPC - 1 && ci == 3 && br == 1) stamp(19);
      v4u V[8];
#pragma unroll
      for (int e = 0; e < 8; ++e) { const LAS unsigned char* s = L + L_RING + ((vr + slot * SLOT) ^ (32u * e)); V[e] = trfrag(s, s + 4096); }
      LDS_WAIT();
      ctl[W_FREE + slot] = (unsigned)(fl + 1);
      PIN8(V);
#pragma unroll
      for (int e = 0; e < 8; ++e) acc[e] = mfma(V[e], pf[t], acc[e]);
    };
    each<0, TPC>(vtile);
    if (c < 8) {
      LAS unsigned char* row = L + L_MRG + (ci * 8 + c) * MROW + g * 16;
#pragma unroll
      for (int e = 0; e < 8; ++e) *(LAS f32x4*)(row + e * 64) = acc[e];
    }
    LDS_WAIT();
    lds_add(W_MCNT + wb);
    const int rec = WS_H1 + ((br * 32 + kvh * 8 + h) * 4 + b * 2 + (l5 >> 4)) * 8192 + part * 256 + (l5 & 15) * 16;
    const int mlo = lane < 8 && ci == 0 ? WS_ML + (br * 8 + sidx) * 4096 + part * 128 + lane * 16 : OOB;
    if (ALATE && br == 1) { st(keep, rec - 32 * 4 * 8192); st(keepl, mlo == OOB ? OOB : mlo - 8 * 4096); }
    wait_ge(W_MCNT + wb, (unsigned)NCONS, IC<0>{});
    const LAS unsigned char* mr = L + L_MRG + h * MROW + l5 * 16;
    const f32x4 o = ((*(const LAS f32x4*)mr + *(const LAS f32x4*)(mr + 8 * MROW)) + (*(const LAS f32x4*)(mr + 16 * MROW) + *(const LAS f32x4*)(mr + 24 * MROW))) * inv;
    keep = v4u{cvtpk(o[0], o[1]), tA, cvtpk(o[2], o[3]), tB};
    keepl = v4u{u(lse), tA, ctl[W_STAMP], tB};
    if (br == 1) hold(0);
    if (!ALATE || br == 1) { st(keep, rec); st(keepl, mlo); }
    if (ci == 3) stamp(br ? 6 : 5);
  }
#pragma unroll 1
  for (int br = 0; br < 2; ++br) {
    spin([&] { return wo_ready(F_WOA + 4 * br); }, IC<1>{});
    v4u wf[8];
    wo_frags(wf, F_WOA + 4 * br, ci);
    project(wf, br, ci, tA, tB);
    if (ci == 0) stamp(br ? 7 : 22);
  }
}

template <int SOFF, int TPM, int HREL, int BEH, int LEAD, int STAMPS>
static void launch(void* const* d_in, void* d_out, void* d_ws, hipStream_t stream) {
  Params p;
  for (int br = 0; br < 2; ++br) {
    p.x[br] = (const u16*)d_in[5 * br + 0]; p.k[br] = (const u16*)d_in[5 * br + 1]; p.v[br] = (const u16*)d_in[5 * br + 2];
    p.wq[br] = (const u16*)d_in[5 * br + 3]; p.wo[br] = (const u16*)d_in[5 * br + 4];
  }
  p.out = (u16*)d_out; p.ws = (unsigned char*)d_ws;
  constexpr int bytes = L_BYTES + (STAMPS ? 256 : 0);
  (void)hipFuncSetAttribute((const void*)k_fused<SOFF, TPM, HREL, BEH, LEAD, STAMPS>, hipFuncAttributeMaxDynamicSharedMemorySize, bytes);
  hipLaunchKernelGGL((k_fused<SOFF, TPM, HREL, BEH, LEAD, STAMPS>), dim3(NWG), dim3(64 * NWAVE), bytes, stream, p);
}

}

extern "C" void kernel_launch(void* const* d_in, const int* in_sizes, int n_in, void* d_out, int out_size, void* d_ws, size_t ws_size,
                              hipStream_t stream) {
  fused::launch<CFG>(d_in, d_out, d_ws, stream);
}
```

```cpp
#include <hip/hip_runtime.h>
#include <hip/hip_bf16.h>
#include <cstdint>

#define GAS __attribute__((address_space(1)))
#define LAS __attribute__((address_space(3)))
typedef unsigned v4u __attribute__((ext_vector_type(4)));
typedef unsigned v2u __attribute__((ext_vector_type(2)));
typedef float f32x4 __attribute__((ext_vector_type(4)));
typedef float f32x2 __attribute__((ext_vector_type(2)));
typedef short bf16x8 __attribute__((ext_vector_type(8)));
typedef short s16x4 __attribute__((ext_vector_type(4)));
typedef unsigned short u16;
typedef unsigned long long u64;

namespace fused {

constexpr int SK = 32768, DH = 128;
constexpr int NWG = 256, NCONS = 4, NWAVE = 10;
constexpr int NSLOT = 16, SLOT = 8192;
constexpr int NT = 32, TPC = NT / NCONS;
constexpr int F_KA = 8, F_VA = F_KA + NT, F_KB = F_VA + NT, F_VB = F_KB + NT, F_WO = F_VB + NT;
constexpr int WN = 16, WX = 16, LAG = WN / 8 + 1;
constexpr float C2 = 0.088388347648318440f * 1.4426950408889634f;
constexpr int OOB = 0x7ffffff0;
constexpr u64 TCAP = 2000000;
constexpr int T_QA = 100, T_QB = 600, T_A = 250;
constexpr int T_SCHED = 70, T_LATE = 20, T_LEAD = 30;
constexpr int T_HOLD = T_SCHED + 170, T_B = 70, T_OUT = 60;

constexpr int WS_EPOCH = 0;
constexpr int WS_ML = 4096;
constexpr int WS_Q = WS_ML + 2 * 8 * 4096;
constexpr int WS_H2 = WS_Q + 2 * 8 * 16384;
constexpr int WS_H1 = WS_H2 + 256 * 4096;
constexpr int WS_END = WS_H1 + 2 * 128 * 8192;

constexpr int L_RING = 0;
constexpr int L_CTL = NSLOT * SLOT;
constexpr int L_QIMG = L_CTL + 512;
constexpr int L_QRED = L_QIMG + 4096;
constexpr int L_OIMG = L_QRED + 2048;
constexpr int L_STAT = L_OIMG + 1024;
constexpr int MROW = 528;
constexpr int L_MRG = L_STAT + 256;
constexpr int L_BYTES = L_MRG + 4 * 8 * MROW;
enum { W_FULL = 0, W_FREE = 16, W_ABORT = 32, W_TAGA, W_TAGB, W_TAGOK, W_STAMP, W_HOLD, W_BASE, W_TAU, W_QCNT, W_QPUB,
       W_QRDY = 48, W_XCNT, W_LCNT, W_MCNT, W_ORDY, W_PER = 8 };
static_assert(L_BYTES <= 160 * 1024 && L_BYTES > 80 * 1024 && (W_BASE & 1) == 0, "one workgroup per CU; {base, tau} is one 8-byte word");

struct Params { const u16* x[2]; const u16* k[2]; const u16* v[2]; const u16* wq[2]; const u16* wo[2]; u16* out; unsigned char* ws; };

__device__ __forceinline__ u64 rt() { return __builtin_amdgcn_s_memrealtime(); }
__device__ __forceinline__ unsigned rfl(unsigned v) { return (unsigned)__builtin_amdgcn_readfirstlane((int)v); }
__device__ __forceinline__ unsigned cvtpk(float lo, float hi) {
  unsigned r; asm("v_cvt_pk_bf16_f32 %0, %1, %2" : "=v"(r) : "v"(lo), "v"(hi)); return r;
}
__device__ __forceinline__ f32x4 mfma(v4u a, v4u b, f32x4 c) {
  return __builtin_amdgcn_mfma_f32_16x16x32_bf16(__builtin_bit_cast(bf16x8, a), __builtin_bit_cast(bf16x8, b), c, 0, 0, 0);
}
__device__ __forceinline__ float f(unsigned u) { return __uint_as_float(u); }
__device__ __forceinline__ unsigned u(float x) { return __float_as_uint(x); }
__device__ __forceinline__ float xrow_max(float x) {
  auto s = __builtin_amdgcn_permlane16_swap(u(x), u(x), false, false);
  x = fmaxf(f(s[0]), f(s[1]));
  auto t = __builtin_amdgcn_permlane32_swap(u(x), u(x), false, false);
  return fmaxf(f(t[0]), f(t[1]));
}
__device__ __forceinline__ float xrow_sum(float x) {
  auto s = __builtin_amdgcn_permlane16_swap(u(x), u(x), false, false);
  x = f(s[0]) + f(s[1]);
  auto t = __builtin_amdgcn_permlane32_swap(u(x), u(x), false, false);
  return f(t[0]) + f(t[1]);
}
template <int CTRL> __device__ __forceinline__ float dpp(float x) {
  return __builtin_bit_cast(float, __builtin_amdgcn_mov_dpp(__builtin_bit_cast(int, x), CTRL, 0xf, 0xf, true));
}
constexpr int ROR1 = 0x121, ROR2 = 0x122, ROR4 = 0x124, ROR8 = 0x128, BCAST = 0x150;
__device__ __forceinline__ float sum32(float x) {
  x += dpp<ROR1>(x); x += dpp<ROR2>(x); x += dpp<ROR4>(x);
  return xrow_sum(x);
}
template <int N> struct IC { static constexpr int value = N; };
template <int I, int E, class F> __device__ __forceinline__ void each(F& fn) {
  if constexpr (I < E) { fn(IC<I>{}); each<I + 1, E>(fn); }
}
template <int N> __device__ __forceinline__ void vmwait() { asm volatile("s_waitcnt vmcnt(%0)" ::"i"(N) : "memory"); }
#define LDS_WAIT() asm volatile("s_waitcnt lgkmcnt(0)" ::: "memory")
#define LDS_ACQ() __builtin_amdgcn_fence(__ATOMIC_ACQUIRE, "workgroup")
#define SCHED() __builtin_amdgcn_sched_barrier(0)
#define PIN8(w) asm volatile("" : "+v"(w[0]), "+v"(w[1]), "+v"(w[2]), "+v"(w[3]), "+v"(w[4]), "+v"(w[5]), "+v"(w[6]), "+v"(w[7]) : : "memory")

__device__ __forceinline__ v4u trfrag(const LAS unsigned char* lo, const LAS unsigned char* hi) {
  const v2u a = __builtin_bit_cast(v2u, __builtin_amdgcn_ds_read_tr16_b64_v4i16((LAS s16x4*)lo));
  const v2u b = __builtin_bit_cast(v2u, __builtin_amdgcn_ds_read_tr16_b64_v4i16((LAS s16x4*)hi));
  return v4u{a[0], a[1], b[0], b[1]};
}

__global__ void __launch_bounds__(64 * NWAVE, 3) k_fused(Params p) {
  extern __shared__ __attribute__((aligned(16))) unsigned char lds_raw[];
  LAS unsigned char* L = (LAS unsigned char*)lds_raw;
  volatile LAS unsigned* ctl = (volatile LAS unsigned*)(L + L_CTL);
  const u64 t_start = rt();
  const int bid = blockIdx.x, sidx = bid >> 5, part = bid & 31, b = sidx >> 2, kvh = sidx & 3;
  const int tid = threadIdx.x, lane = tid & 63, wave = (int)rfl(tid >> 6);
  const int c = lane & 15, g = lane >> 4;

  if (__builtin_expect(wave >= 1 && wave <= NCONS, 1)) {
    const unsigned char* wq = (const unsigned char*)p.wq[bid >> 7] + (size_t)(((bid >> 6) & 1) * 512 + (wave - 1) * 128) * 8192 + (bid & 63) * 128;
    const unsigned off = (unsigned)(lane >> 3) * 8192u + (unsigned)(lane & 7) * 16u;
#pragma unroll
    for (int j = 0; j < 16; ++j)
      __builtin_amdgcn_global_load_lds((const GAS unsigned*)(wq + (size_t)j * 8 * 8192 + off), (LAS unsigned*)(L + L_RING + (wave - 1) * 2 * SLOT + j * 1024), 16, 0, 2);
  }

  auto spin = [&](auto&& cond, auto nap) {
    for (unsigned it = 1; !cond(); ++it) {
      if constexpr (decltype(nap)::value > 0) __builtin_amdgcn_s_sleep(decltype(nap)::value);
      if ((it & 63u) == 0u) {
        if (rt() - t_start > TCAP) ctl[W_ABORT] = 1u;
        if (rfl(ctl[W_ABORT])) break;
      }
    }
  };
  auto wait_ge = [&](int w, unsigned n, auto nap) {
    spin([&] { return rfl(ctl[w]) >= n; }, nap);
    LDS_ACQ();
  };

  if (__builtin_expect(wave == 0, 1)) {
    __builtin_amdgcn_s_setprio(3);
    ctl[lane] = 0u;
    unsigned koff[4], voff[2];
#pragma unroll
    for (int j = 0; j < 4; ++j) koff[j] = (unsigned)g * 256u + (unsigned)((c ^ (4 * j + g)) * 16);
#pragma unroll
    for (int j = 0; j < 2; ++j) voff[j] = (unsigned)g * 256u + (unsigned)((c ^ (2 * (4 * j + g))) * 16);
    int fill = F_KA;
    unsigned freed = 0u, now = 0u;
    v2u sched = {0u, 0u};
    auto full = [&](int fl) { ctl[W_FULL + (fl & (NSLOT - 1))] = (unsigned)(fl + 1); };
    auto drain = [&]() {
      static_assert(LAG + WX / 8 <= 6, "drain covers the widest window");
      vmwait<40>(); full(fill - 6); vmwait<32>(); full(fill - 5); vmwait<24>(); full(fill - 4);
      vmwait<16>(); full(fill - 3); vmwait<8>(); full(fill - 2); vmwait<0>(); full(fill - 1);
    };
    auto issue = [&](const unsigned char* s0, const unsigned char* s1, unsigned o0, unsigned o1, unsigned o2, unsigned o3) {
      const unsigned dst = (unsigned)(size_t)(L + L_RING) + (unsigned)((fill & (NSLOT - 1)) * SLOT);
      if (rfl(freed) < (unsigned)(fill - NSLOT + 1) && fill >= NSLOT) {
        drain();
        spin([&] { return rfl(ctl[W_FREE + (fill & (NSLOT - 1))]) >= (unsigned)(fill - NSLOT + 1); }, IC<1>{});
      }
      bool wide = false;
      const unsigned tau = rfl(sched[1]);
      if (tau != 0u && fill < F_WO) {
        const unsigned due = rfl(sched[0]) + ((unsigned)fill * tau >> 8);
        int late = (int)(now - due);
        wide = late > T_LATE;
        while (late < -T_LEAD && late > -1000) { __builtin_amdgcn_s_sleep(1); late = (int)((unsigned)rt() - due); }
      }
#define FILL(N) asm volatile("s_mov_b32 m0, %0\n\ts_waitcnt vmcnt(%7)\n\t"                                                     \
                   "global_load_lds_dwordx4 %3, %1 nt\n\tglobal_load_lds_dwordx4 %4, %1 offset:1024 nt\n\t"                    \
                   "global_load_lds_dwordx4 %5, %1 offset:2048 nt\n\tglobal_load_lds_dwordx4 %6, %1 offset:3072 nt\n\t"        \
                   "s_add_u32 m0, %0, 0x1000\n\ts_nop 0\n\t"                                                                  \
                   "global_load_lds_dwordx4 %3, %2 nt\n\tglobal_load_lds_dwordx4 %4, %2 offset:1024 nt\n\t"                    \
                   "global_load_lds_dwordx4 %5, %2 offset:2048 nt\n\tglobal_load_lds_dwordx4 %6, %2 offset:3072 nt"            \
                   :: "s"(dst), "s"(s0), "s"(s1), "v"(o0), "v"(o1), "v"(o2), "v"(o3), "i"(N) : "memory", "scc")
      if (wide) {
        FILL(WN + WX);
        full(fill - LAG - WX / 8);
      } else {
        FILL(WN);
#pragma unroll
        for (int j = WX / 8; j >= 0; --j) full(fill - LAG - j);
      }
#undef FILL
      ++fill;
      freed = ctl[W_FREE + (fill & (NSLOT - 1))];
      sched = *(volatile LAS v2u*)&ctl[W_BASE];
      now = (unsigned)rt();
    };
    const size_t sbase = (size_t)sidx * SK * DH * 2;
    auto tile_off = [&](int i) -> size_t { return (size_t)((((((i >> 1) + sidx) & 15) * 32 + part) << 1) | (i & 1)) * 8192; };
    const size_t wo_base = (size_t)((bid & 31) * 128) * 2048 + (bid >> 5) * 256;
    const unsigned woff = (unsigned)g * 2048u + (unsigned)c * 16u;
    LDS_WAIT();
    __builtin_amdgcn_s_barrier();
#pragma unroll 1
    for (int br = 0; br < 2; ++br) {
      const unsigned char* kb = (const unsigned char*)p.k[br] + sbase;
      const unsigned char* vb = (const unsigned char*)p.v[br] + sbase;
#pragma unroll 1
      for (int i = 0; i < NT; ++i) { const unsigned char* s = kb + tile_off(i); issue(s, s + 4096, koff[0], koff[1], koff[2], koff[3]); }
      const unsigned tk = (unsigned)rt();
#pragma unroll 1
      for (int i = 0; i < NT; ++i) { const unsigned char* s = vb + tile_off(i); issue(s, s + 4096, voff[0], voff[1], voff[0], voff[1]); }
      if (br == 0) { const unsigned tv = (unsigned)rt(); ctl[W_STAMP] = (tv << 12) | ((tv - tk) & 0xfffu); }
    }
#pragma unroll 1
    for (int i = 0; i < 8; ++i) {
      const unsigned char* s = (const unsigned char*)p.wo[i >> 2] + wo_base + (size_t)((i & 3) * 32) * 2048;
      issue(s, s + 4 * 8192, woff, woff + 7168u, woff + 14336u, woff + 21504u);
    }
    drain();
    return;
  }

  const __amdgpu_buffer_rsrc_t rs = __builtin_amdgcn_make_buffer_rsrc(p.ws, 0, WS_END, 0x00020000);
  auto ld = [&](int off) -> v4u { return __builtin_amdgcn_raw_buffer_load_b128(rs, off, 0, 16); };
  auto st = [&](v4u v, int off) { __builtin_amdgcn_raw_buffer_store_b128(v, rs, off, 0, 16); };
  auto lds_add = [&](int w) { if (lane == 0) __hip_atomic_fetch_add((LAS unsigned*)&ctl[w], 1u, __ATOMIC_RELAXED, __HIP_MEMORY_SCOPE_WORKGROUP); };
  auto delay = [&](u64 ticks) { const u64 t0 = rt(); while (rt() - t0 < ticks) __builtin_amdgcn_s_sleep(2); };
  auto hold = [&](int extra) {
    const unsigned h = rfl(ctl[W_HOLD]);
    if (h != 0u) {
      for (;;) {
        const int left = ((int)(h - ((unsigned)rt() << 12)) >> 12) + extra;
        if (left <= 0 || left > 800) break;
        __builtin_amdgcn_s_sleep(1);
      }
    }
  };
  auto poll = [&](auto& v, auto& off, unsigned tA, unsigned tB, auto nap) {
    constexpr int N = sizeof(v) / sizeof(v[0]);
#pragma unroll
    for (int j = 0; j < N; ++j) v[j] = v2u{0u, 0u};
    spin([&] {
      v4u t[N];
#pragma unroll
      for (int j = 0; j < N; ++j) t[j] = ld(off[j]);
      SCHED();
      bool pend = false;
#pragma unroll
      for (int j = 0; j < N; ++j) {
        const bool hit = (t[j][1] == tA) & (t[j][3] == tB) & (off[j] != OOB);
        v[j][0] = hit ? t[j][0] : v[j][0];
        v[j][1] = hit ? t[j][2] : v[j][1];
        off[j] = hit ? OOB : off[j];
        pend = pend | (off[j] != OOB);
      }
      return __any(pend) == 0;
    }, nap);
  };
  const f32x4 zf = {0.f, 0.f, 0.f, 0.f};

  if (wave == NCONS + 1) {
    const u64 ep = __hip_atomic_load((GAS u64*)(p.ws + WS_EPOCH), __ATOMIC_RELAXED, __HIP_MEMORY_SCOPE_AGENT);
    __builtin_amdgcn_s_barrier();
    const unsigned tA = rfl((unsigned)ep) + 1u, tB = rfl((unsigned)(ep >> 32)) + 1u;
    ctl[W_TAGA] = tA; ctl[W_TAGB] = tB;
    LDS_WAIT();
    ctl[W_TAGOK] = 1u;
    wait_ge(W_ORDY + W_PER, (unsigned)NCONS, IC<2>{});
    if (bid & 1) delay(T_OUT);
    v2u r[4];
    int off[4];
#pragma unroll
    for (int j = 0; j < 4; ++j) off[j] = WS_H2 + ((bid & 1) * 128 + (bid >> 1)) * 4096 + lane * 16 + 1024 * j;
    poll(r, off, tA, tB, IC<0>{});
    float s0 = (f(r[0][0]) + f(r[1][0])) + (f(r[2][0]) + f(r[3][0])), s1 = (f(r[0][1]) + f(r[1][1])) + (f(r[2][1]) + f(r[3][1]));
    s0 += dpp<ROR8>(s0); s1 += dpp<ROR8>(s1);
    s0 = xrow_sum(s0); s1 = xrow_sum(s1);
    if (lane < 8) {
      const int col = 32 * (bid >> 3) + 16 * ((lane >> 1) & 1) + 4 * ((bid >> 1) & 3) + 2 * (lane >> 2);
      *(GAS unsigned*)(p.out + (lane & 1) * 2048 + (bid & 1) * 1024 + col) = cvtpk(s0, s1);
    }
    if (bid == 1 && lane == 0)
      __hip_atomic_store((GAS u64*)(p.ws + WS_EPOCH), ((u64)tB << 32) | tA, __ATOMIC_RELAXED, __HIP_MEMORY_SCOPE_AGENT);
    return;
  }

  if (wave > NCONS + 1) {
    const int tw = wave - NCONS - 2;
    __builtin_amdgcn_s_barrier();
    wait_ge(W_QPUB, 1u, IC<2>{});
    const unsigned tA = rfl(ctl[W_TAGA]), tB = rfl(ctl[W_TAGB]);
#pragma unroll 1
    for (int br = 0; br < 2; ++br) {
      delay(br ? T_QB : T_QA);
      v2u q[4];
      int off[4];
#pragma unroll
      for (int j = 0; j < 4; ++j) off[j] = WS_Q + ((br * 4 + kvh) * 2 + b) * 16384 + tw * 2048 + lane * 16 + (j >> 1) * 8192 + (j & 1) * 1024;
      poll(q, off, tA, tB, IC<4>{});
      LAS unsigned* qi = (LAS unsigned*)(L + L_QIMG + br * 2048 + tw * 512 + lane * 4);
      qi[0] = cvtpk(f(q[0][0]) + f(q[2][0]), f(q[0][1]) + f(q[2][1]));
      qi[64] = cvtpk(f(q[1][0]) + f(q[3][0]), f(q[1][1]) + f(q[3][1]));
      LDS_WAIT();
      lds_add(W_QRDY + W_PER * br);
    }
#pragma unroll 1
    for (int br = 0; br < 2; ++br) {
      v2u d[9];
      int off[9];
#pragma unroll
      for (int j = 0; j < 8; ++j) off[j] = WS_H1 + ((br * 32 + (bid & 31)) * 4 + tw) * 8192 + lane * 16 + 1024 * j;
      off[8] = WS_ML + (br * 8 + (tw >> 1) * 4 + ((bid >> 3) & 3)) * 4096 + (4 * (c & 7) + g) * 128 + (bid & 7) * 16;
      if (br == 0) {
        wait_ge(W_MCNT, (unsigned)NCONS, IC<8>{});
        delay(T_A);
        poll(d, off, tA, tB, IC<16>{});
      } else {
        wait_ge(W_MCNT + W_PER, (unsigned)NCONS, IC<2>{});
        hold(T_B);
        poll(d, off, tA, tB, IC<0>{});
      }
      if (br == 0 && tw == 0) {
        const unsigned mine = rfl(ctl[W_STAMP]) & ~0xfffu;
        const int lag = (int)f(rfl(u(sum32((float)((int)(mine - (d[8][1] & ~0xfffu)) >> 12))))) / 32;
        const float per = f(rfl(u(sum32((float)(d[8][1] & 0xfffu))))) * (1.0f / (32 * NT));
        const unsigned now = (unsigned)rt();
        const unsigned mean = now - (unsigned)((int)((now << 12) - mine) >> 12) - (unsigned)lag;
        const unsigned tau = (unsigned)(per * 256.0f);
        *(volatile LAS v2u*)&ctl[W_BASE] = v2u{mean + (unsigned)T_SCHED - ((unsigned)F_KB * tau >> 8), tau};
        ctl[W_HOLD] = ((mean + (unsigned)((int)(per * (float)(F_WO - F_KB)) + T_HOLD)) << 12) | 1u;
      }
      float M = f(d[8][0]);
      M = fmaxf(M, dpp<ROR1>(M)); M = fmaxf(M, dpp<ROR2>(M)); M = fmaxf(M, dpp<ROR4>(M));
      M = xrow_max(M);
      const float w = __builtin_amdgcn_exp2f(f(d[8][0]) - M);
      const float inv = 1.0f / sum32(w);
      f32x4 o = zf;
#define ACC(J) o += f32x4{f(d[J][0] << 16), f(d[J][0] & 0xffff0000u), f(d[J][1] << 16), f(d[J][1] & 0xffff0000u)} * dpp<BCAST + J>(w);
      ACC(0) ACC(1) ACC(2) ACC(3) ACC(4) ACC(5) ACC(6) ACC(7)
#undef ACC
      const v2u pk = {cvtpk(xrow_sum(o[0]) * inv, xrow_sum(o[1]) * inv), cvtpk(xrow_sum(o[2]) * inv, xrow_sum(o[3]) * inv)};
      if (lane < 16) *(LAS v2u*)(L + L_OIMG + br * 512 + tw * 128 + lane * 8) = pk;
      LDS_WAIT();
      lds_add(W_ORDY + W_PER * br);
    }
    return;
  }

  const int ci = wave - 1;
  const int tq = (lane >> 2) & 3, tp = lane & 3;
  LAS float* smax = (LAS float*)(L + L_STAT);
  LAS float* ssum = smax + 32;
  v4u xf[4];
#pragma unroll
  for (int ks = 0; ks < 4; ++ks) xf[ks] = *(const GAS v4u*)(p.x[bid >> 7] + (c & 1) * 1024 + ((bid >> 6) & 1) * 512 + ci * 128 + ks * 32 + 8 * g);
  __builtin_amdgcn_s_barrier();
  vmwait<0>();
  {
    const LAS unsigned char* s = L + L_RING + ci * 2 * SLOT + (8 * g + tq) * 128 + tp * 8;
    v4u w0[8], w1[8];
#pragma unroll
    for (int j = 0; j < 8; ++j) {
      const int o = (j >> 2) * 4096 + (j & 3) * 32;
      w0[j] = trfrag(s + o, s + o + 512); w1[j] = trfrag(s + 8192 + o, s + 8192 + o + 512);
    }
    LDS_WAIT();
    if (lane < 2) ctl[W_FREE + 2 * ci + lane] = (unsigned)(2 * ci + lane + 1);
    PIN8(w0); PIN8(w1);
    auto block = [&](int e) { return mfma(w1[4 + e], xf[3], mfma(w1[e], xf[2], mfma(w0[4 + e], xf[1], mfma(w0[e], xf[0], zf)))); };
    const f32x4 a0 = block(0), a1 = block(1), a2 = block(2), a3 = block(3);
    const f32x4 lo = (c & 2) ? a1 : a0, hi = (c & 2) ? a3 : a2, r = (c & 4) ? hi : lo;
    if (c < 8) *(LAS f32x4*)(L + L_QRED + ci * 512 + (c & 1) * 256 + (c >> 1) * 64 + g * 16) = r;
    LDS_WAIT();
    lds_add(W_QCNT);
  }
  wait_ge(W_TAGOK, 1u, IC<0>{});
  const unsigned tA = rfl(ctl[W_TAGA]), tB = rfl(ctl[W_TAGB]);
  if (ci == 0) {
    wait_ge(W_QCNT, (unsigned)NCONS, IC<0>{});
    f32x2 q = {0.f, 0.f};
#pragma unroll
    for (int w = 0; w < NCONS; ++w) q += *(const LAS f32x2*)(L + L_QRED + w * 512 + lane * 8);
    st(v4u{u(q[0]), tA, u(q[1]), tB},
       WS_Q + ((((bid >> 7) * 4 + ((bid >> 4) & 3)) * 2 + (lane >> 5)) * 2 + ((bid >> 6) & 1)) * 8192 + (bid & 15) * 512 + (lane & 31) * 16);
    ctl[W_QPUB] = 1u;
  }

#pragma unroll 1
  for (int br = 0; br < 2; ++br) {
    const int wb = W_PER * br, fk = (br ? F_KB : F_KA) + ci, fv = (br ? F_VB : F_VA) + ci;
    f32x4 sc[TPC][2];
    v4u qf[4];
    unsigned kr = (unsigned)c * 256u + (unsigned)((g ^ c) * 16);
    asm volatile("" : "+v"(kr));
    auto ktile = [&](auto tc) {
      constexpr int t = decltype(tc)::value;
      const int fl = fk + NCONS * t, slot = fl & (NSLOT - 1);
      wait_ge(W_FULL + slot, (unsigned)(fl + 1), IC<4>{});
      v4u K[8];
#pragma unroll
      for (int j = 0; j < 8; ++j) K[j] = *(const LAS v4u*)(L + L_RING + ((kr + slot * SLOT) ^ (64u * (j & 3))) + (j >> 2) * 4096);
      LDS_WAIT();
      ctl[W_FREE + slot] = (unsigned)(fl + 1);
      PIN8(K);
      if constexpr (t == 0) {
        wait_ge(W_QRDY + wb, (unsigned)NCONS, IC<1>{});
#pragma unroll
        for (int j = 0; j < 4; ++j) qf[j] = *(const LAS v4u*)(L + L_QIMG + br * 2048 + (c & 7) * 256 + j * 64 + g * 16);
      }
      f32x4 s0 = zf, s1 = zf;
#pragma unroll
      for (int j = 0; j < 4; ++j) { s0 = mfma(K[j], qf[j], s0); s1 = mfma(K[4 + j], qf[j], s1); }
      sc[t][0] = s0; sc[t][1] = s1;
    };
    each<0, TPC>(ktile);
    {
      float m = -INFINITY;
#pragma unroll
      for (int t = 0; t < TPC; ++t)
#pragma unroll
        for (int j = 0; j < 2; ++j) m = fmaxf(m, fmaxf(fmaxf(sc[t][j][0], sc[t][j][1]), fmaxf(sc[t][j][2], sc[t][j][3])));
      m = xrow_max(m) * C2;
      if (lane < 8) smax[ci * 8 + lane] = m;
      LDS_WAIT();
      lds_add(W_XCNT + wb);
    }
    wait_ge(W_XCNT + wb, (unsigned)NCONS, IC<0>{});
    const int h8 = lane & 7;
    const float m = fmaxf(fmaxf(smax[h8], smax[8 + h8]), fmaxf(smax[16 + h8], smax[24 + h8]));
    v4u pf[TPC];
    {
      float l = 0.f;
#pragma unroll
      for (int t = 0; t < TPC; ++t) {
        float pr[8];
#pragma unroll
        for (int i = 0; i < 8; ++i) pr[i] = __builtin_amdgcn_exp2f(fmaf(sc[t][i >> 2][i & 3], C2, -m));
        pf[t] = v4u{cvtpk(pr[0], pr[1]), cvtpk(pr[2], pr[3]), cvtpk(pr[4], pr[5]), cvtpk(pr[6], pr[7])};
        l += ((pr[0] + pr[1]) + (pr[2] + pr[3])) + ((pr[4] + pr[5]) + (pr[6] + pr[7]));
      }
      l = xrow_sum(l);
      if (lane < 8) ssum[ci * 8 + lane] = l;
      LDS_WAIT();
      lds_add(W_LCNT + wb);
    }
    const int h = 2 * ci + (lane >> 5), l5 = lane & 31;
    wait_ge(W_LCNT + wb, (unsigned)NCONS, IC<0>{});
    const float inv = 1.0f / ((ssum[h] + ssum[8 + h]) + (ssum[16 + h] + ssum[24 + h]));
    const float lse = m + __builtin_amdgcn_logf((ssum[h8] + ssum[8 + h8]) + (ssum[16 + h8] + ssum[24 + h8]));
    f32x4 acc[8];
#pragma unroll
    for (int e = 0; e < 8; ++e) acc[e] = zf;
    unsigned vr = (unsigned)(4 * g + tq) * 256u + (unsigned)((4 * (g & 1) + tq) * 32 + tp * 8);
    asm volatile("" : "+v"(vr));
    auto vtile = [&](auto tc) {
      constexpr int t = decltype(tc)::value;
      const int fl = fv + NCONS * t, slot = fl & (NSLOT - 1);
      wait_ge(W_FULL + slot, (unsigned)(fl + 1), IC<(t == TPC - 1 ? 1 : 4)>{});
      v4u V[8];
#pragma unroll
      for (int e = 0; e < 8; ++e) { const LAS unsigned char* s = L + L_RING + ((vr + slot * SLOT) ^ (32u * e)); V[e] = trfrag(s, s + 4096); }
      LDS_WAIT();
      ctl[W_FREE + slot] = (unsigned)(fl + 1);
      PIN8(V);
#pragma unroll
      for (int e = 0; e < 8; ++e) acc[e] = mfma(V[e], pf[t], acc[e]);
    };
    each<0, TPC>(vtile);
    if (c < 8) {
      LAS unsigned char* row = L + L_MRG + (ci * 8 + c) * MROW + g * 16;
#pragma unroll
      for (int e = 0; e < 8; ++e) *(LAS f32x4*)(row + e * 64) = acc[e];
    }
    LDS_WAIT();
    lds_add(W_MCNT + wb);
    const int rec = WS_H1 + ((br * 32 + kvh * 8 + h) * 4 + b * 2 + (l5 >> 4)) * 8192 + part * 256 + (l5 & 15) * 16;
    const int mlo = lane < 8 && ci == 0 ? WS_ML + (br * 8 + sidx) * 4096 + part * 128 + lane * 16 : OOB;
    wait_ge(W_MCNT + wb, (unsigned)NCONS, IC<0>{});
    const LAS unsigned char* mr = L + L_MRG + h * MROW + l5 * 16;
    const f32x4 o = ((*(const LAS f32x4*)mr + *(const LAS f32x4*)(mr + 8 * MROW)) + (*(const LAS f32x4*)(mr + 16 * MROW) + *(const LAS f32x4*)(mr + 24 * MROW))) * inv;
    const unsigned stamp = ctl[W_STAMP];
    if (br == 1) hold(0);
    st(v4u{cvtpk(o[0], o[1]), tA, cvtpk(o[2], o[3]), tB}, rec);
    st(v4u{u(lse), tA, stamp, tB}, mlo);
  }

#pragma unroll 1
  for (int br = 0; br < 2; ++br) {
    const int f0 = F_WO + 4 * br;
    wait_ge(W_FULL + ((f0 + 3) & (NSLOT - 1)), (unsigned)(f0 + 4), IC<1>{});
    v4u wf[8];
    const LAS unsigned char* s = L + L_RING + (f0 & (NSLOT - 1)) * SLOT + (8 * g + tq) * 256 + ci * 64 + tp * 8;
#pragma unroll
    for (int j = 0; j < 8; ++j) wf[j] = trfrag(s + (j >> 1) * 8192 + (j & 1) * 32, s + (j >> 1) * 8192 + (j & 1) * 32 + 1024);
    const int o2 = c < 8 ? WS_H2 + (br * 128 + ((bid >> 5) * 4 + ci) * 4 + g) * 4096 + (bid & 31) * 128 + c * 16 : OOB;
    wait_ge(W_ORDY + W_PER * br, (unsigned)NCONS, IC<0>{});
    f32x4 a0 = zf, a1 = zf;
#pragma unroll
    for (int ks = 0; ks < 4; ++ks) {
      const v4u of = *(const LAS v4u*)(L + L_OIMG + br * 512 + (c & 1) * 256 + ks * 64 + g * 16);
      a0 = mfma(wf[2 * ks], of, a0);
      a1 = mfma(wf[2 * ks + 1], of, a1);
    }
    const f32x4 r = (c & 2) ? a1 : a0;
    st(v4u{u((c & 4) ? r[2] : r[0]), tA, u((c & 4) ? r[3] : r[1]), tB}, o2);
  }
}

}

extern "C" void kernel_launch(void* const* d_in, const int* in_sizes, int n_in, void* d_out, int out_size, void* d_ws, size_t ws_size,
                              hipStream_t stream) {
  using namespace fused;
  Params p;
  for (int br = 0; br < 2; ++br) {
    p.x[br] = (const u16*)d_in[5 * br + 0]; p.k[br] = (const u16*)d_in[5 * br + 1]; p.v[br] = (const u16*)d_in[5 * br + 2];
    p.wq[br] = (const u16*)d_in[5 * br + 3]; p.wo[br] = (const u16*)d_in[5 * br + 4];
  }
  p.out = (u16*)d_out; p.ws = (unsigned char*)d_ws;
  (void)hipFuncSetAttribute((const void*)k_fused, hipFuncAttributeMaxDynamicSharedMemorySize, L_BYTES);
  hipLaunchKernelGGL(k_fused, dim3(NWG), dim3(64 * NWAVE), L_BYTES, stream, p);
}
```
